```python
import torch

def _softmax(x, axis=-1):
    return torch.softmax(x, dim=axis)
def _gelu(x):
    return torch.nn.functional.gelu(x, approximate="tanh")
def _silu(x):
    return torch.nn.functional.silu(x)
def _sigmoid(x):
    return torch.sigmoid(x)
def _layernorm(x, axis=-1, eps=1e-5):
    m = x.mean(dim=axis, keepdim=True)
    v = x.var(dim=axis, keepdim=True, unbiased=False)
    return (x - m) / torch.sqrt(v + eps)
def _scatter_add(tgt, idx, src, axis=0):
    return tgt.clone().index_add_(axis, idx, src)
def _segment_sum(src, seg, n):
    out = torch.zeros((n,) + src.shape[1:], dtype=src.dtype)
    return out.index_add_(0, seg, src)
def _pad(x, pw):
    flat = tuple(v for pair in reversed(pw) for v in pair)
    return torch.nn.functional.pad(x, flat)

kernel_name = 'interleave_layers__ssm_conv_s4d__dense_activation_mish__prompt'

def setup_inputs():

    _w = (lambda a: (lambda t: t.to(torch.bfloat16) if t.is_floating_point() else t)(torch.as_tensor(a)))
    def _A_setup():
        Bsz, L, D, N = 1, 2048, 512, 128
        return {
            "u": _w((lambda *s: torch.randn(*s, dtype=torch.bfloat16) * 0.05)(Bsz, L, D)),
            "log_A_real": _w((lambda *s: torch.randn(*s, dtype=torch.bfloat16) * 3.2)(D, N) * 0.5),
            "A_imag": _w((lambda *s: torch.randn(*s, dtype=torch.bfloat16) * 0.05)(D, N) * 3.14),
            "C_real": _w((lambda *s: torch.randn(*s, dtype=torch.bfloat16) * 0.05)(D, N) / N ** 0.5),
            "C_imag": _w((lambda *s: torch.randn(*s, dtype=torch.bfloat16) * 0.05)(D, N) / N ** 0.5),
            "log_dt": _w((lambda *s: torch.randn(*s, dtype=torch.bfloat16) * 0.05)(D) * 0.5 - 4.0),
            "D_skip": _w((lambda *s: torch.randn(*s, dtype=torch.bfloat16) * 0.05)(D)),
        }
    _A_in = _A_setup()

    def _B_setup():
        B, S, D, F = 1, 2048, 512, 1792
        return {
            "x": _w((lambda *s: torch.randn(*s, dtype=torch.bfloat16) * 0.05)(B, S, D)),
            "w1": _w((lambda *s: torch.randn(*s, dtype=torch.bfloat16) * 0.8)(D, F) * (D ** -0.5)),
            "b1": _w((lambda *s: torch.randn(*s, dtype=torch.bfloat16) * 0.2)(F)),
            "w2": _w((lambda *s: torch.randn(*s, dtype=torch.bfloat16) * 0.2)(F, D) * (F ** -0.5)),
        }
    _B_in = _B_setup()

    return {
        "A_u": _A_in["u"],
        "A_log_A_real": _A_in["log_A_real"],
        "A_A_imag": _A_in["A_imag"],
        "A_C_real": _A_in["C_real"],
        "A_C_imag": _A_in["C_imag"],
        "A_log_dt": _A_in["log_dt"],
        "A_D_skip": _A_in["D_skip"],
        "B_x": _B_in["x"],
        "B_w1": _B_in["w1"],
        "B_b1": _B_in["b1"],
        "B_w2": _B_in["w2"],
    }

def reference(A_u, A_log_A_real, A_A_imag, A_C_real, A_C_imag, A_log_dt, A_D_skip, B_x, B_w1, B_b1, B_w2):
    def _A_ref(u, log_A_real, A_imag, C_real, C_imag, log_dt, D_skip):
        L = u.shape[1]
        dt = torch.exp(log_dt)
        A = -torch.exp(log_A_real) + 1j * A_imag
        C = (C_real + 1j * C_imag) * dt[:, None]
        pos = (lambda x, dt: x.to(dt))(torch.arange(L), log_dt.dtype)
        P = torch.exp((A * dt[:, None])[:, :, None] * pos[None, None, :])
        K = 2.0 * torch.einsum("dn,dnl->dl", C, P).real
        L2 = 2 * L
        u_t = (lambda x, *d: x.permute(*d))(u, 0, 2, 1)
        Kf = (lambda x, *a, **k: torch.fft.rfft(x.float(), *a, **k))(K, L2)
        Uf = (lambda x, *a, **k: torch.fft.rfft(x.float(), *a, **k))(u_t, L2)
        y = (lambda x, *a, **k: torch.fft.irfft(x, *a, **k).to(torch.bfloat16))(Uf * Kf[None, :, :], L2)[:, :, :L]
        y = (lambda x, *d: x.permute(*d))(y, 0, 2, 1)
        return y + u * D_skip[None, None, :]

    def _B_ref(x, w1, b1, w2):
        h = torch.einsum("bsd,df->bsf", x, w1) + b1
        sp = torch.log1p(torch.exp(-torch.abs(h))) + torch.where(h > 0, h, torch.zeros_like(h))
        act = h * torch.tanh(sp)
        return torch.einsum("bsf,fd->bsd", act, w2)

    x = A_u
    x = x + _A_ref(_layernorm(x, axis=-1), A_log_A_real, A_A_imag, A_C_real, A_C_imag, A_log_dt, A_D_skip)
    x = x + _B_ref(_layernorm(x, axis=-1), B_w1, B_b1, B_w2)
    x = x + _A_ref(_layernorm(x, axis=-1), A_log_A_real, A_A_imag, A_C_real, A_C_imag, A_log_dt, A_D_skip)
    x = x + _B_ref(_layernorm(x, axis=-1), B_w1, B_b1, B_w2)
    return x
```

```cpp
#include <hip/hip_runtime.h>
#include <hip/hip_bf16.h>
typedef __hip_bfloat16 bf16;
constexpr int L=2048, D=512, N=128, F=1792;
__device__ __forceinline__ float b2f(bf16 x){return __bfloat162float(x);}
__device__ __forceinline__ float rb(float x){return __bfloat162float(__float2bfloat16(x));}

__global__ void kgen(const bf16* lar,const bf16* aim,const bf16* cr,const bf16* ci,const bf16* ldt,float* K){
  int d=blockIdx.x;
  __shared__ float ar[N],ai[N],Cr[N],Ci[N];
  float dt=rb(expf(b2f(ldt[d])));
  if(threadIdx.x<N){int n=threadIdx.x; ar[n]=-rb(expf(b2f(lar[d*N+n])))*dt; ai[n]=b2f(aim[d*N+n])*dt; Cr[n]=b2f(cr[d*N+n])*dt; Ci[n]=b2f(ci[d*N+n])*dt;}
  __syncthreads();
  for(int l=threadIdx.x;l<L;l+=blockDim.x){
    float p=rb((float)l); float s=0;
    for(int n=0;n<N;n++){float e=expf(ar[n]*p); float sn,cs; sincosf(ai[n]*p,&sn,&cs); s+=e*(Cr[n]*cs-Ci[n]*sn);}
    K[d*L+l]=2.f*s;
  }
}
__global__ void ln(const bf16* x, bf16* u){
  int r=blockIdx.x; int t=threadIdx.x;
  float v[8]; float s=0;
  for(int i=0;i<8;i++){v[i]=b2f(x[r*D+t*8+i]); s+=v[i];}
  for(int o=32;o;o>>=1)s+=__shfl_xor(s,o);
  float m=s/D; float q=0;
  for(int i=0;i<8;i++){q+=(v[i]-m)*(v[i]-m);}
  for(int o=32;o;o>>=1)q+=__shfl_xor(q,o);
  float rs=rsqrtf(q/D+1e-5f);
  for(int i=0;i<8;i++)u[r*D+t*8+i]=__float2bfloat16((v[i]-m)*rs);
}
__global__ void conv(const bf16* x,const bf16* u,const float* K,const bf16* dsk,bf16* xo){
  int d=blockIdx.x; __shared__ float sk[L],su[L];
  for(int l=threadIdx.x;l<L;l+=blockDim.x){sk[l]=K[d*L+l];su[l]=b2f(u[l*D+d]);}
  __syncthreads();
  float ds=b2f(dsk[d]);
  for(int l=threadIdx.x;l<L;l+=blockDim.x){
    float s=0; for(int j=0;j<=l;j++)s+=sk[j]*su[l-j];
    xo[l*D+d]=__float2bfloat16(b2f(x[l*D+d])+s+su[l]*ds);
  }
}
__global__ void g1(const bf16* u,const bf16* w1,const bf16* b1,bf16* h){
  int r=blockIdx.x; __shared__ float su[D];
  for(int i=threadIdx.x;i<D;i+=blockDim.x)su[i]=b2f(u[r*D+i]);
  __syncthreads();
  for(int f=threadIdx.x;f<F;f+=blockDim.x){
    float s=0; for(int k=0;k<D;k++)s+=su[k]*b2f(w1[k*F+f]);
    s+=b2f(b1[f]);
    float sp=log1pf(expf(-fabsf(s)))+fmaxf(s,0.f);
    h[r*F+f]=__float2bfloat16(s*tanhf(sp));
  }
}
__global__ void g2(const bf16* x,const bf16* h,const bf16* w2,bf16* xo){
  int r=blockIdx.x; __shared__ float sh[F];
  for(int i=threadIdx.x;i<F;i+=blockDim.x)sh[i]=b2f(h[r*F+i]);
  __syncthreads();
  for(int c=threadIdx.x;c<D;c+=blockDim.x){
    float s=0; for(int k=0;k<F;k++)s+=sh[k]*b2f(w2[k*D+c]);
    xo[r*D+c]=__float2bfloat16(b2f(x[r*D+c])+s);
  }
}
extern "C" void kernel_launch(void* const* d_in, const int* in_sizes, int n_in, void* d_out, int out_size, void* d_ws, size_t ws_size, hipStream_t stream){
  const bf16* u0=(const bf16*)d_in[0];
  char* ws=(char*)d_ws;
  float* K=(float*)ws; bf16* u=(bf16*)(ws+(4<<20)); bf16* x1=(bf16*)(ws+(6<<20)); bf16* x2=(bf16*)(ws+(8<<20)); bf16* h=(bf16*)(ws+(10<<20));
  kgen<<<D,256,0,stream>>>((const bf16*)d_in[1],(const bf16*)d_in[2],(const bf16*)d_in[3],(const bf16*)d_in[4],(const bf16*)d_in[5],K);
  const bf16* x=u0;
  for(int it=0;it<2;it++){
    ln<<<L,64,0,stream>>>(x,u);
    conv<<<D,256,0,stream>>>(x,u,K,(const bf16*)d_in[6],x1);
    ln<<<L,64,0,stream>>>(x1,u);
    g1<<<L,256,0,stream>>>(u,(const bf16*)d_in[8],(const bf16*)d_in[9],h);
    bf16* o = it==0? x2 : (bf16*)d_out;
    g2<<<L,256,0,stream>>>(x1,h,(const bf16*)d_in[10],o);
    x=x2;
  }
}
```

```cpp
#include <hip/hip_runtime.h>
#include <hip/hip_bf16.h>
#include <stdint.h>

#define GAS __attribute__((address_space(1)))
#define LAS __attribute__((address_space(3)))
typedef unsigned v4u __attribute__((ext_vector_type(4)));
typedef unsigned v2u __attribute__((ext_vector_type(2)));
typedef float f32x4 __attribute__((ext_vector_type(4)));
typedef float f32x2 __attribute__((ext_vector_type(2)));
typedef double f64x2 __attribute__((ext_vector_type(2)));
typedef __bf16 bf16x8 __attribute__((ext_vector_type(8)));
typedef __bf16 bf16x2 __attribute__((ext_vector_type(2)));
typedef _Float16 f16x8 __attribute__((ext_vector_type(8)));
typedef _Float16 f16x2 __attribute__((ext_vector_type(2)));
typedef unsigned char u8;

constexpr int L_ = 2048, D_ = 512, N_ = 128, F_ = 1792;

constexpr int KC_STRIDE = 4160, KC_CH = 4 * KC_STRIDE, KC_PAIR = 33792;
constexpr size_t OFF_CTRL = 0;
constexpr size_t OFF_ST0 = 65536, OFF_ST1 = OFF_ST0 + 32768;
constexpr size_t OFF_KSC = OFF_ST1 + 32768;
constexpr size_t OFF_CS = OFF_KSC + 4096;
constexpr size_t OFF_B1 = OFF_CS + 8192;
constexpr size_t OFF_KC = 262144;
constexpr size_t XP_BYTES = (size_t)L_ * D_ * 2;
constexpr size_t OFF_XP0 = OFF_KC + 256 * (size_t)KC_PAIR;
constexpr size_t OFF_XP1 = OFF_XP0 + XP_BYTES, OFF_XP2 = OFF_XP1 + XP_BYTES, OFF_XP3 = OFF_XP2 + XP_BYTES;
constexpr size_t W_BYTES = (size_t)D_ * F_ * 2;
constexpr size_t OFF_W1F = OFF_XP3 + XP_BYTES, OFF_W2F = OFF_W1F + W_BYTES;
constexpr size_t H_BYTES = (size_t)L_ * F_ * 2;
constexpr size_t OFF_H0 = OFF_W2F + W_BYTES, OFF_H1 = OFF_H0 + H_BYTES;
constexpr size_t WS_BYTES = OFF_H1 + H_BYTES;
static_assert(WS_BYTES < (1ull << 31), "int offsets");

constexpr int RSRC_W3 = 0x00020000;

__device__ __forceinline__ float bf_lo(unsigned u) { return __builtin_bit_cast(float, u << 16); }
__device__ __forceinline__ float bf_hi(unsigned u) { return __builtin_bit_cast(float, u & 0xFFFF0000u); }
__device__ __forceinline__ float bf1(unsigned short u) { return __builtin_bit_cast(float, (unsigned)u << 16); }
__device__ __forceinline__ unsigned pk_bf16(float a, float b) {
  bf16x2 v = {(__bf16)a, (__bf16)b};
  return __builtin_bit_cast(unsigned, v);
}
__device__ __forceinline__ unsigned pk_f16(float a, float b) {
  f16x2 v = {(_Float16)a, (_Float16)b};
  return __builtin_bit_cast(unsigned, v);
}
__device__ __forceinline__ float rbf(float a) { return (float)(__bf16)a; }
__device__ __forceinline__ f32x4 mfma_bf(v4u a, v4u b, f32x4 c) {
  return __builtin_amdgcn_mfma_f32_16x16x32_bf16(__builtin_bit_cast(bf16x8, a), __builtin_bit_cast(bf16x8, b), c, 0, 0, 0);
}
__device__ __forceinline__ f32x4 mfma_h(v4u a, v4u b, f32x4 c) {
  return __builtin_amdgcn_mfma_f32_16x16x32_f16(__builtin_bit_cast(f16x8, a), __builtin_bit_cast(f16x8, b), c, 0, 0, 0);
}
__device__ __forceinline__ float dot2bf(unsigned a, unsigned b, float c) {
  return __builtin_amdgcn_fdot2_f32_bf16(__builtin_bit_cast(bf16x2, a), __builtin_bit_cast(bf16x2, b), c, false);
}
__device__ __forceinline__ void glds16(const void* gsrc, unsigned lds_dst) {
  unsigned keep;
  asm volatile("s_mov_b32 %0, m0\n\ts_mov_b32 m0, %2\n\ts_nop 0\n\tglobal_load_lds_dwordx4 %1, off\n\ts_mov_b32 m0, %0"
               : "=&s"(keep) : "v"(gsrc), "s"(lds_dst) : "memory");
}
#define WAITV(n) asm volatile("s_waitcnt vmcnt(" #n ")" ::: "memory")
#define LDS_BAR() do { asm volatile("s_waitcnt lgkmcnt(0)" ::: "memory"); __builtin_amdgcn_s_barrier(); asm volatile("" ::: "memory"); } while (0)

struct cpx { float r, i; };
__device__ __forceinline__ cpx cmul(cpx a, cpx b) { return {a.r * b.r - a.i * b.i, a.r * b.i + a.i * b.r}; }
__device__ __forceinline__ cpx cexp_m(float ar, float ai, float m) {
  float mag = __builtin_amdgcn_exp2f(ar * m * 1.44269504088896f);
  float rev = __builtin_amdgcn_fractf(ai * m * 0.159154943091895f);
  return {mag * __builtin_amdgcn_cosf(rev), mag * __builtin_amdgcn_sinf(rev)};
}

constexpr int GS_ROW = 132;
constexpr int PREP_GS = 0;
constexpr int PREP_ES = 2 * 48 * GS_ROW * 4;
constexpr int PREP_KP = PREP_ES + 2 * 16 * GS_ROW * 4;
constexpr int PREP_LDS = PREP_KP + 2176 * 4;

__device__ __forceinline__ void kgen_body(LAS u8* lds, int pair, const unsigned short* lar_, const unsigned short* aim_,
                                          const unsigned short* cr_, const unsigned short* ci_, const unsigned short* ldt_, u8* ws) {
  const int t = threadIdx.x, lane = t & 63, w = __builtin_amdgcn_readfirstlane(t >> 6);
  const int ch = t >> 7, n = t & 127, d = 2 * pair + ch;
  const float lar = bf1(lar_[d * N_ + n]), aim = bf1(aim_[d * N_ + n]);
  const float cr = bf1(cr_[d * N_ + n]), ci = bf1(ci_[d * N_ + n]);
  const float cr2 = bf1(cr_[d * N_ + (n ^ 64)]), ci2 = bf1(ci_[d * N_ + (n ^ 64)]);
  const float dt = rbf(expf(bf1(ldt_[d])));
  const float ar = -rbf(expf(lar)) * dt, ai = aim * dt;
  float mx = fmaxf(fmaxf(fabsf(cr), fabsf(ci)), fmaxf(fabsf(cr2), fabsf(ci2))) * dt;
#pragma unroll
  for (int o = 32; o; o >>= 1) mx = fmaxf(mx, __shfl_xor(mx, o));
  unsigned pb = __builtin_bit_cast(unsigned, mx) & 0x7F800000u;
  if (pb == 0u || pb >= 0x7E800000u) pb = 0x3F800000u;
  const float P2 = __builtin_bit_cast(float, pb);
  const float inv = __builtin_bit_cast(float, 0x7F000000u - pb);
  if ((t & 127) == 0) ((float*)(ws + OFF_KSC))[d] = 2.0f * P2;
  const cpx z1 = cexp_m(ar, ai, 1.f), z8 = cexp_m(ar, ai, 8.f), z128 = cexp_m(ar, ai, 128.f);
  LAS unsigned* Gs = (LAS unsigned*)(lds + PREP_GS) + ch * 48 * GS_ROW + n;
  LAS unsigned* Es = (LAS unsigned*)(lds + PREP_ES) + ch * 16 * GS_ROW + n;
  LAS unsigned* Kp = (LAS unsigned*)(lds + PREP_KP);
  if (t == 0) Kp[2049] = 0u;
  {
    cpx e = {1.f, 0.f};
#pragma unroll
    for (int l = 0; l < 16; ++l) { Es[l * GS_ROW] = pk_f16(e.r, e.i); if (l < 15) e = (l == 0) ? z8 : cmul(e, z8); }
  }
  cpx Q[8];
  Q[1] = z1;
#pragma unroll
  for (int o = 2; o < 8; ++o) Q[o] = cmul(Q[o - 1], z1);
  cpx P = {cr * dt * inv, ci * dt * inv};
#pragma unroll
  for (int H = 0; H <= 16; ++H) {
    const int step = H < 2 ? 1 : H < 4 ? 2 : H < 8 ? 4 : 8;
    const int row0 = H < 2 ? 8 * H : H < 4 ? 16 + 4 * (H - 2) : H < 8 ? 24 + 2 * (H - 4) : 32 + (H - 8);
#pragma unroll
    for (int o = 0; o < 8; o += step) {
      cpx g = (o == 0) ? P : cmul(P, Q[o]);
      Gs[(row0 + o / step) * GS_ROW] = pk_f16(g.r, -g.i);
    }
    if (H < 16) P = cmul(P, z128);
  }
  LDS_BAR();
  {
    const int r = lane & 15, h = lane >> 4;
    for (int task = w; task < 6; task += 4) {
      const int tch = task & 1, mt = task >> 1;
      const LAS v4u* ga = (const LAS v4u*)(lds + PREP_GS + ((tch * 48 + mt * 16 + r) * GS_ROW + 4 * h) * 4);
      const LAS v4u* eb = (const LAS v4u*)(lds + PREP_ES + ((tch * 16 + r) * GS_ROW + 4 * h) * 4);
      f32x4 acc = {0.f, 0.f, 0.f, 0.f};
#pragma unroll
      for (int ks = 0; ks < 8; ++ks) acc = mfma_h(ga[ks * 4], eb[ks * 4], acc);
#pragma unroll
      for (int reg = 0; reg < 4; ++reg) {
        const int row = mt * 16 + 4 * h + reg;
        int off;
        if (row < 16) off = 128 * (row >> 3) + (row & 7);
        else if (row < 24) off = 256 + 128 * ((row - 16) >> 2) + 2 * (row & 3);
        else if (row < 32) off = 512 + 128 * ((row - 24) >> 1) + 4 * (row & 1);
        else off = 1024 + 128 * (row - 32);
        if (row <= 40) {
          _Float16 hv = (_Float16)acc[reg];
          *(LAS _Float16*)(lds + PREP_KP + (off + 8 * r) * 4 + 2 * tch) = hv;
        }
      }
    }
  }
  LDS_BAR();
  {
    unsigned v[11];
#pragma unroll
    for (int i = 0; i < 11; ++i) {
      const int lag = 2047 - (8 * t + i);
      unsigned u = __builtin_bit_cast(unsigned, (float)lag);
      u = (u + 0x7FFFu + ((u >> 16) & 1u)) & 0xFFFF0000u;
      int p = (int)__builtin_bit_cast(float, u);
      p = lag < 0 ? 2049 : p;
      v[i] = Kp[p];
    }
    u8* dst = ws + OFF_KC + (size_t)pair * KC_PAIR + 16 * t;
#pragma unroll
    for (int c2 = 0; c2 < 2; ++c2) {
      unsigned pr[10];
#pragma unroll
      for (int i = 0; i < 10; ++i) pr[i] = __builtin_amdgcn_perm(v[i + 1], v[i], c2 ? 0x07060302u : 0x05040100u);
#pragma unroll
      for (int c = 0; c < 4; ++c) *(v4u*)(dst + c2 * KC_CH + c * KC_STRIDE) = v4u{pr[c], pr[c + 2], pr[c + 4], pr[c + 6]};
    }
    if (t < 32) *(v4u*)(ws + OFF_KC + (size_t)pair * KC_PAIR + (t >> 2) * KC_STRIDE + 4096 + (t & 3) * 16) = v4u{0u, 0u, 0u, 0u};
  }
}

__device__ __forceinline__ void ln1_body(LAS u8* lds, int b, const unsigned short* x0, u8* ws) {
  const int t = threadIdx.x, lane = t & 63, w = t >> 6, tok = lane & 15, q = lane >> 4;
  const int l = 16 * b + tok, c0 = 128 * w + 32 * q;
  v4u v[4];
#pragma unroll
  for (int i = 0; i < 4; ++i) v[i] = *(const v4u*)(x0 + (size_t)l * D_ + c0 + 8 * i);
  float s = 0.f, ss = 0.f;
  unsigned* xp = (unsigned*)(ws + OFF_XP0);
#pragma unroll
  for (int i = 0; i < 4; ++i)
#pragma unroll
    for (int j = 0; j < 4; ++j) {
      const unsigned u = v[i][j];
      s = dot2bf(u, 0x3F803F80u, s);
      ss = dot2bf(u, u, ss);
      xp[(size_t)(c0 / 2 + 4 * i + j) * L_ + l] = u;
    }
  s += __shfl_xor(s, 16); ss += __shfl_xor(ss, 16);
  s += __shfl_xor(s, 32); ss += __shfl_xor(ss, 32);
  LAS f32x2* red = (LAS f32x2*)lds;
  if (lane < 16) red[w * 16 + lane] = f32x2{s, ss};
  LDS_BAR();
  if (t < 16) {
    double a = 0.0, c = 0.0;
#pragma unroll
    for (int i = 0; i < 4; ++i) { f32x2 r = red[i * 16 + t]; a += (double)r.x; c += (double)r.y; }
    ((f64x2*)(ws + OFF_ST0))[16 * b + t] = f64x2{a, c};
    ((f64x2*)(ws + OFF_ST1))[16 * b + t] = f64x2{0.0, 0.0};
  }
}

__device__ __forceinline__ void tr8x8(const v4u (&r)[8], v4u (&o)[8]) {
#pragma unroll
  for (int e = 0; e < 8; ++e)
#pragma unroll
    for (int jj = 0; jj < 4; ++jj)
      o[e][jj] = __builtin_amdgcn_perm(r[2 * jj + 1][e >> 1], r[2 * jj][e >> 1], (e & 1) ? 0x07060302u : 0x05040100u);
}

__device__ __forceinline__ void w1_body(LAS u8* lds, int b, const unsigned short* w1, const unsigned short* b1, u8* ws) {
  const int t = threadIdx.x, lane = t & 63, w = t >> 6, dg = t >> 2, fq = t & 3;
  v4u r[8], o[8];
#pragma unroll
  for (int j = 0; j < 8; ++j) r[j] = *(const v4u*)(w1 + (size_t)(8 * dg + j) * F_ + 32 * b + 8 * fq);
  tr8x8(r, o);
  const int phi = 2 * b + (fq >> 1), cb = phi / 7, ph = phi % 7, ks = dg >> 2, hq = dg & 3;
  u8* dst = ws + OFF_W1F + ((size_t)((cb * 16 + ks) * 7 + ph) * 64 + 8 * (fq & 1) + 16 * hq) * 16;
  float cs[8];
#pragma unroll
  for (int e = 0; e < 8; ++e) {
    *(v4u*)(dst + e * 16) = o[e];
    float s = 0.f;
#pragma unroll
    for (int jj = 0; jj < 4; ++jj) s = dot2bf(o[e][jj], 0x3F803F80u, s);
#pragma unroll
    for (int x = 4; x < 64; x <<= 1) s += __shfl_xor(s, x);
    cs[e] = s;
  }
  LAS float* red = (LAS float*)lds;
  if (lane < 4) {
#pragma unroll
    for (int e = 0; e < 8; ++e) red[w * 32 + 8 * lane + e] = cs[e];
  }
  LDS_BAR();
  if (t < 32) {
    ((float*)(ws + OFF_CS))[32 * b + t] = (red[t] + red[32 + t]) + (red[64 + t] + red[96 + t]);
    ((float*)(ws + OFF_B1))[32 * b + t] = bf1(b1[32 * b + t]);
  }
}

__device__ __forceinline__ void w2_body(int ks2, const unsigned short* w2, u8* ws) {
  const int t = threadIdx.x, h = t >> 6, dq = t & 63;
  v4u r[8], o[8];
#pragma unroll
  for (int j = 0; j < 8; ++j) r[j] = *(const v4u*)(w2 + (size_t)(32 * ks2 + 16 * (j >> 2) + 4 * h + (j & 3)) * D_ + 8 * dq);
  tr8x8(r, o);
  const int db = dq >> 3, dd0 = (8 * dq) & 63;
#pragma unroll
  for (int e = 0; e < 8; ++e) {
    const int dd = dd0 + e, dt = 2 * (dd >> 5) + (dd & 1), c = (dd & 31) >> 1;
    *(v4u*)(ws + OFF_W2F + ((size_t)((db * 56 + ks2) * 4 + dt) * 64 + c + 16 * h) * 16) = o[e];
  }
}

__global__ void __launch_bounds__(256) k_prep(const unsigned short* x0, const unsigned short* lar, const unsigned short* aim,
                                              const unsigned short* cr, const unsigned short* ci, const unsigned short* ldt,
                                              const unsigned short* w1, const unsigned short* b1, const unsigned short* w2, u8* ws) {
  extern __shared__ __attribute__((aligned(16))) u8 smem[];
  LAS u8* lds = (LAS u8*)smem;
  const int b = blockIdx.x;
  if (b < 256) kgen_body(lds, b, lar, aim, cr, ci, ldt, ws);
  else if (b < 384) ln1_body(lds, b - 256, x0, ws);
  else if (b < 440) w1_body(lds, b - 384, w1, b1, ws);
  else w2_body(b - 440, w2, ws);
}

constexpr int U_CH = (256 + 2048) * 2;
constexpr int CV_K = 0, CV_U = KC_PAIR, CV_X = CV_U + 2 * U_CH, CV_LDS = CV_X + 32768;

__global__ void __launch_bounds__(256, 1) k_conv(u8* ws, const unsigned short* dskip, int off_xin, int off_st, int off_xout) {
  extern __shared__ __attribute__((aligned(16))) u8 smem[];
  LAS u8* lds = (LAS u8*)smem;
  const int t = threadIdx.x, lane = t & 63, w = __builtin_amdgcn_readfirstlane(t >> 6);
  const int pair = blockIdx.x;
  const int a = lane & 15, h = lane >> 4;
  const unsigned lds0 = (unsigned)(uintptr_t)lds;
  {
    const u8* src = ws + OFF_KC + (size_t)pair * KC_PAIR + lane * 16;
    for (int p = w; p < 33; p += 4) glds16(src + p * 1024, lds0 + CV_K + p * 1024);
  }
  const int tok0 = 512 * w + 16 * a + 4 * h;
  v4u xv[2];
  f64x2 st[2][4];
#pragma unroll
  for (int i = 0; i < 2; ++i) {
    xv[i] = *(const v4u*)(ws + off_xin + ((size_t)pair * L_ + tok0 + 256 * i) * 4);
#pragma unroll
    for (int j = 0; j < 4; ++j) st[i][j] = *(const f64x2*)(ws + off_st + (size_t)(tok0 + 256 * i + j) * 16);
  }
  const unsigned dsk = *(const unsigned*)(dskip + 2 * pair);
  const f32x2 ksc = *(const f32x2*)(ws + OFF_KSC + pair * 8);
  if (t < 64) *(LAS v4u*)(lds + CV_U + (t >> 5) * U_CH + (t & 31) * 16) = v4u{0u, 0u, 0u, 0u};
  const float ds0 = bf_lo(dsk), ds1 = bf_hi(dsk);
  float base[2][2][4];
#pragma unroll
  for (int i = 0; i < 2; ++i) {
    float u0[4], u1[4];
#pragma unroll
    for (int j = 0; j < 4; ++j) {
      const double m = st[i][j].x * (1.0 / D_);
      const double var = st[i][j].y * (1.0 / D_) - m * m;
      const float r = __builtin_amdgcn_rsqf(fmaxf((float)var, 0.f) + 1e-5f);
      const float mf = (float)m;
      const float x0 = bf_lo(xv[i][j]), x1 = bf_hi(xv[i][j]);
      u0[j] = (x0 - mf) * r; u1[j] = (x1 - mf) * r;
      base[i][0][j] = x0 + u0[j] * ds0; base[i][1][j] = x1 + u1[j] * ds1;
    }
    const int ub = CV_U + (256 + tok0 + 256 * i) * 2;
    *(LAS v2u*)(lds + ub) = v2u{pk_f16(u0[0], u0[1]), pk_f16(u0[2], u0[3])};
    *(LAS v2u*)(lds + ub + U_CH) = v2u{pk_f16(u1[0], u1[1]), pk_f16(u1[2], u1[3])};
  }
  WAITV(0);
  LDS_BAR();
  const int ch = w >> 1, s0b = 4 * (w & 1);
  const int cc = (3 - a) & 3;
  const LAS u8* pa = lds + CV_K + ch * KC_CH + cc * KC_STRIDE + 2 * (15 - a + 8 * h - cc) + 64 * (63 - 56 - 3 - s0b);
  const LAS u8* pb = lds + CV_U + ch * U_CH + 480 + 32 * a + 16 * h - 64 * (s0b + 3);
  f32x4 acc[8];
#pragma unroll
  for (int i = 0; i < 8; ++i) acc[i] = f32x4{0.f, 0.f, 0.f, 0.f};
#pragma unroll
  for (int si = 0; si < 4; ++si) {
    v4u A[8], B[8];
#pragma unroll
    for (int g = 0; g < 8; ++g) {
      const int oa = 64 * ((3 - si) + 8 * (7 - g));
      const v2u lo = *(const LAS v2u*)(pa + oa), hi = *(const LAS v2u*)(pa + oa + 8);
      A[g] = v4u{lo.x, lo.y, hi.x, hi.y};
      B[g] = *(const LAS v4u*)(pb + 64 * (3 - si) + 512 * g);
    }
#pragma unroll
    for (int g = 0; g < 8; ++g)
#pragma unroll
      for (int tp = 0; tp + g < 8; ++tp) acc[tp + g] = mfma_h(A[g], B[tp], acc[tp + g]);
  }
  LAS f32x4* ex = (LAS f32x4*)(lds + CV_X);
#pragma unroll
  for (int i = 0; i < 8; ++i) ex[(w * 8 + i) * 64 + lane] = acc[i];
  LDS_BAR();
#pragma unroll
  for (int i = 0; i < 2; ++i) {
    const f32x4 y0 = ex[(0 * 8 + 2 * w + i) * 64 + lane] + ex[(1 * 8 + 2 * w + i) * 64 + lane];
    const f32x4 y1 = ex[(2 * 8 + 2 * w + i) * 64 + lane] + ex[(3 * 8 + 2 * w + i) * 64 + lane];
    v4u o;
#pragma unroll
    for (int j = 0; j < 4; ++j) o[j] = pk_bf16(base[i][0][j] + y0[j] * ksc.x, base[i][1][j] + y1[j] * ksc.y);
    *(v4u*)(ws + off_xout + ((size_t)pair * L_ + tok0 + 256 * i) * 4) = o;
  }
}

constexpr int G1_LDS = 112 * 1024;

__device__ __forceinline__ float mish(float hh) {
  const float n = __builtin_amdgcn_exp2f(fminf(hh * 1.44269504088896f, 40.f));
  const float p = n * (n + 2.f);
  return hh * p * __builtin_amdgcn_rcpf(p + 2.f);
}

__global__ void __launch_bounds__(256, 1) k_g1(u8* ws, int off_x, int off_h) {
  extern __shared__ __attribute__((aligned(16))) u8 smem[];
  LAS u8* lds = (LAS u8*)smem;
  const int t = threadIdx.x, lane = t & 63, w = __builtin_amdgcn_readfirstlane(t >> 6);
  const int xcd = blockIdx.x & 7, idx = blockIdx.x >> 3;
  const int rb = 4 * (xcd >> 1) + (idx & 3), cb = 8 * (xcd & 1) + (idx >> 2);
  const int c = lane & 15, h = lane >> 4;
  const unsigned lds0 = (unsigned)(uintptr_t)lds;
  const __amdgpu_buffer_rsrc_t rs = __builtin_amdgcn_make_buffer_rsrc((void*)ws, 0, (int)WS_BYTES, RSRC_W3);
  const int g = 4 * rb + w;
  const int xoff = (4 * h * L_ + 32 * g + 2 * c) * 4;
  const u8* wsrc = ws + OFF_W1F + (size_t)cb * G1_LDS + lane * 16;
  v2u X[16][4];
#pragma unroll
  for (int q = 0; q < 4; ++q) {
#pragma unroll
    for (int i = 0; i < 7; ++i) { const int p = 28 * q + 4 * i + w; glds16(wsrc + p * 1024, lds0 + p * 1024); }
#pragma unroll
    for (int k = 0; k < 4; ++k)
#pragma unroll
      for (int i = 0; i < 4; ++i)
        X[4 * q + k][i] = __builtin_amdgcn_raw_buffer_load_b64(rs, xoff, off_x + ((16 * (4 * q + k) + i) * L_) * 4, 0);
    __builtin_amdgcn_sched_barrier(0);
  }
  f32x4 acc[7][2];
#pragma unroll
  for (int i = 0; i < 7; ++i) { acc[i][0] = f32x4{0.f, 0.f, 0.f, 0.f}; acc[i][1] = f32x4{0.f, 0.f, 0.f, 0.f}; }
  float s0 = 0.f, s1 = 0.f, q0 = 0.f, q1 = 0.f;
  const LAS v4u* wl = (const LAS v4u*)(lds + lane * 16);
#pragma unroll
  for (int q = 0; q < 4; ++q) {
    if (q == 0) WAITV(63); else if (q == 1) WAITV(46); else if (q == 2) WAITV(23); else WAITV(0);
    __builtin_amdgcn_s_barrier();
    asm volatile("" ::: "memory");
#pragma unroll
    for (int k = 0; k < 4; ++k) {
      const int ks = 4 * q + k;
      const v4u b0 = v4u{X[ks][0].x, X[ks][1].x, X[ks][2].x, X[ks][3].x};
      const v4u b1 = v4u{X[ks][0].y, X[ks][1].y, X[ks][2].y, X[ks][3].y};
#pragma unroll
      for (int i = 0; i < 7; ++i) {
        const v4u av = wl[(ks * 7 + i) * 64];
        acc[i][0] = mfma_bf(av, b0, acc[i][0]);
        acc[i][1] = mfma_bf(av, b1, acc[i][1]);
      }
#pragma unroll
      for (int i = 0; i < 4; ++i) {
        s0 = dot2bf(b0[i], 0x3F803F80u, s0); q0 = dot2bf(b0[i], b0[i], q0);
        s1 = dot2bf(b1[i], 0x3F803F80u, s1); q1 = dot2bf(b1[i], b1[i], q1);
      }
    }
  }
  s0 += __shfl_xor(s0, 16); q0 += __shfl_xor(q0, 16); s1 += __shfl_xor(s1, 16); q1 += __shfl_xor(q1, 16);
  s0 += __shfl_xor(s0, 32); q0 += __shfl_xor(q0, 32); s1 += __shfl_xor(s1, 32); q1 += __shfl_xor(q1, 32);
  float r[2], rm[2];
  {
    const float m0 = s0 * (1.f / D_), m1 = s1 * (1.f / D_);
    r[0] = __builtin_amdgcn_rsqf(fmaxf(q0 * (1.f / D_) - m0 * m0, 0.f) + 1e-5f);
    r[1] = __builtin_amdgcn_rsqf(fmaxf(q1 * (1.f / D_) - m1 * m1, 0.f) + 1e-5f);
    rm[0] = -r[0] * m0; rm[1] = -r[1] * m1;
  }
  v2u o[7][2];
#pragma unroll
  for (int i = 0; i < 7; ++i) {
    const int f0 = 112 * cb + 16 * i + 4 * h;
    const f32x4 cs = *(const f32x4*)(ws + OFF_CS + f0 * 4);
    const f32x4 bb = *(const f32x4*)(ws + OFF_B1 + f0 * 4);
#pragma unroll
    for (int tt = 0; tt < 2; ++tt) {
      float a[4];
#pragma unroll
      for (int j = 0; j < 4; ++j) a[j] = mish(fmaf(r[tt], acc[i][tt][j], fmaf(rm[tt], cs[j], bb[j])));
      o[i][tt] = v2u{pk_bf16(a[0], a[1]), pk_bf16(a[2], a[3])};
    }
  }
  const int tb = g >> 1;
  const int hbase = off_h + ((tb * 56) * 4 + 2 * (g & 1)) * 1024;
  const int k0 = (7 * cb) >> 1;
#pragma unroll
  for (int tt = 0; tt < 2; ++tt) {
    if ((cb & 1) == 0) {
#pragma unroll
      for (int i = 0; i < 3; ++i)
        __builtin_amdgcn_raw_buffer_store_b128(v4u{o[2 * i][tt].x, o[2 * i][tt].y, o[2 * i + 1][tt].x, o[2 * i + 1][tt].y}, rs, lane * 16,
                                               hbase + ((k0 + i) * 4 + tt) * 1024, 0);
      __builtin_amdgcn_raw_buffer_store_b64(o[6][tt], rs, lane * 16, hbase + ((k0 + 3) * 4 + tt) * 1024, 0);
    } else {
      __builtin_amdgcn_raw_buffer_store_b64(o[0][tt], rs, lane * 16 + 8, hbase + (k0 * 4 + tt) * 1024, 0);
#pragma unroll
      for (int i = 0; i < 3; ++i)
        __builtin_amdgcn_raw_buffer_store_b128(v4u{o[2 * i + 1][tt].x, o[2 * i + 1][tt].y, o[2 * i + 2][tt].x, o[2 * i + 2][tt].y}, rs, lane * 16,
                                               hbase + ((k0 + 1 + i) * 4 + tt) * 1024, 0);
    }
  }
}

constexpr int G2_LDS = 65536;
constexpr int G2_WIN = 6;

__global__ void __launch_bounds__(256, 1) k_g2(u8* ws, int off_h, int off_xres, int off_xout, unsigned* out, int last) {
  extern __shared__ __attribute__((aligned(16))) u8 smem[];
  LAS u8* lds = (LAS u8*)smem;
  const int t = threadIdx.x, lane = t & 63, w = __builtin_amdgcn_readfirstlane(t >> 6);
  const int xcd = blockIdx.x & 7, idx = blockIdx.x >> 3;
  const int tb = 8 * (xcd >> 1) + (idx & 7), db = 4 * (xcd & 1) + (idx >> 3);
  const int c = lane & 15, h = lane >> 4;
  const __amdgpu_buffer_rsrc_t rs = __builtin_amdgcn_make_buffer_rsrc((void*)ws, 0, (int)WS_BYTES, RSRC_W3);
  const int hb = off_h + (tb * 56 + 14 * w) * 4096;
  const int wb = (int)OFF_W2F + (db * 56 + 14 * w) * 4096;
  v4u A[G2_WIN][4], B[G2_WIN][4];
#pragma unroll
  for (int k = 0; k < G2_WIN; ++k) {
#pragma unroll
    for (int i = 0; i < 4; ++i) {
      A[k][i] = __builtin_amdgcn_raw_buffer_load_b128(rs, lane * 16 + i * 1024, hb + k * 4096, 0);
      B[k][i] = __builtin_amdgcn_raw_buffer_load_b128(rs, lane * 16 + i * 1024, wb + k * 4096, 0);
    }
  }
  const int gsel = w >> 1, dsel = w & 1;
  const int pair = 32 * db + 16 * dsel + c, tok0 = 64 * tb + 32 * gsel + 8 * h;
  v4u xr[2];
  xr[0] = __builtin_amdgcn_raw_buffer_load_b128(rs, (pair * L_ + tok0) * 4, off_xres, 0);
  xr[1] = __builtin_amdgcn_raw_buffer_load_b128(rs, (pair * L_ + tok0) * 4 + 16, off_xres, 0);
  __builtin_amdgcn_sched_barrier(0);
  f32x4 acc[4][4];
#pragma unroll
  for (int i = 0; i < 4; ++i)
#pragma unroll
    for (int j = 0; j < 4; ++j) acc[i][j] = f32x4{0.f, 0.f, 0.f, 0.f};
#pragma unroll
  for (int k = 0; k < 14; ++k) {
    const int s = k % G2_WIN;
#pragma unroll
    for (int i = 0; i < 4; ++i)
#pragma unroll
      for (int j = 0; j < 4; ++j) acc[i][j] = mfma_bf(A[s][i], B[s][j], acc[i][j]);
    __builtin_amdgcn_sched_barrier(0);
    if (k + G2_WIN < 14) {
#pragma unroll
      for (int i = 0; i < 4; ++i) {
        A[s][i] = __builtin_amdgcn_raw_buffer_load_b128(rs, lane * 16 + i * 1024, hb + (k + G2_WIN) * 4096, 0);
        B[s][i] = __builtin_amdgcn_raw_buffer_load_b128(rs, lane * 16 + i * 1024, wb + (k + G2_WIN) * 4096, 0);
      }
    }
    __builtin_amdgcn_sched_barrier(0);
  }
  LAS f32x4* red = (LAS f32x4*)lds;
#pragma unroll
  for (int i = 0; i < 4; ++i)
#pragma unroll
    for (int j = 0; j < 4; ++j) red[((w * 4 + i) * 4 + j) * 64 + lane] = acc[i][j];
  LDS_BAR();
  f32x4 y[2][2];
#pragma unroll
  for (int i = 0; i < 2; ++i)
#pragma unroll
    for (int j = 0; j < 2; ++j) {
      const int e = ((2 * gsel + i) * 4 + 2 * dsel + j) * 64 + lane;
      y[i][j] = (red[e] + red[1024 + e]) + (red[2048 + e] + red[3072 + e]);
    }
  unsigned o[8];
#pragma unroll
  for (int k = 0; k < 8; ++k) {
    const unsigned xx = xr[k >> 2][k & 3];
    o[k] = pk_bf16(bf_lo(xx) + y[k & 1][0][k >> 1], bf_hi(xx) + y[k & 1][1][k >> 1]);
  }
  if (last) {
#pragma unroll
    for (int k = 0; k < 8; ++k) out[(size_t)(tok0 + k) * (D_ / 2) + pair] = o[k];
  } else {
    __builtin_amdgcn_raw_buffer_store_b128(v4u{o[0], o[1], o[2], o[3]}, rs, (pair * L_ + tok0) * 4, off_xout, 0);
    __builtin_amdgcn_raw_buffer_store_b128(v4u{o[4], o[5], o[6], o[7]}, rs, (pair * L_ + tok0) * 4 + 16, off_xout, 0);
    float v[16];
#pragma unroll
    for (int k = 0; k < 8; ++k) { v[2 * k] = dot2bf(o[k], 0x3F803F80u, 0.f); v[2 * k + 1] = dot2bf(o[k], o[k], 0.f); }
#pragma unroll
    for (int st = 8; st >= 1; st >>= 1) {
      const bool up = (c & st) != 0;
#pragma unroll
      for (int i = 0; i < st; ++i) {
        const float keep = up ? v[i + st] : v[i], send = up ? v[i] : v[i + st];
        v[i] = keep + __shfl_xor(send, st);
      }
    }
    double* sp = (double*)(ws + OFF_ST1) + (size_t)tok0 * 2 + c;
    __builtin_amdgcn_global_atomic_fadd_f64((GAS double*)sp, (double)v[0]);
  }
}

extern "C" void kernel_launch(void* const* d_in, const int* in_sizes, int n_in, void* d_out, int out_size, void* d_ws, size_t ws_size,
                              hipStream_t stream) {
  typedef const unsigned short* cus;
  u8* ws = (u8*)d_ws;
  (void)hipFuncSetAttribute((const void*)k_prep, hipFuncAttributeMaxDynamicSharedMemorySize, PREP_LDS);
  (void)hipFuncSetAttribute((const void*)k_conv, hipFuncAttributeMaxDynamicSharedMemorySize, CV_LDS);
  (void)hipFuncSetAttribute((const void*)k_g1, hipFuncAttributeMaxDynamicSharedMemorySize, G1_LDS);
  (void)hipFuncSetAttribute((const void*)k_g2, hipFuncAttributeMaxDynamicSharedMemorySize, G2_LDS);
  k_prep<<<496, 256, PREP_LDS, stream>>>((cus)d_in[0], (cus)d_in[1], (cus)d_in[2], (cus)d_in[3], (cus)d_in[4], (cus)d_in[5], (cus)d_in[8],
                                         (cus)d_in[9], (cus)d_in[10], ws);
  k_conv<<<256, 256, CV_LDS, stream>>>(ws, (cus)d_in[6], (int)OFF_XP0, (int)OFF_ST0, (int)OFF_XP1);
  k_g1<<<256, 256, G1_LDS, stream>>>(ws, (int)OFF_XP1, (int)OFF_H0);
  k_g2<<<256, 256, G2_LDS, stream>>>(ws, (int)OFF_H0, (int)OFF_XP1, (int)OFF_XP2, (unsigned*)d_out, 0);
  k_conv<<<256, 256, CV_LDS, stream>>>(ws, (cus)d_in[6], (int)OFF_XP2, (int)OFF_ST1, (int)OFF_XP3);
  k_g1<<<256, 256, G1_LDS, stream>>>(ws, (int)OFF_XP3, (int)OFF_H1);
  k_g2<<<256, 256, G2_LDS, stream>>>(ws, (int)OFF_H1, (int)OFF_XP3, (int)OFF_XP2, (unsigned*)d_out, 1);
}
```

```cpp
#include <hip/hip_runtime.h>
#include <hip/hip_bf16.h>
#include <stdint.h>

#define GAS __attribute__((address_space(1)))
#define LAS __attribute__((address_space(3)))
typedef unsigned v4u __attribute__((ext_vector_type(4)));
typedef unsigned v2u __attribute__((ext_vector_type(2)));
typedef float f32x4 __attribute__((ext_vector_type(4)));
typedef float f32x2 __attribute__((ext_vector_type(2)));
typedef __bf16 bf16x8 __attribute__((ext_vector_type(8)));
typedef __bf16 bf16x2 __attribute__((ext_vector_type(2)));
typedef _Float16 f16x8 __attribute__((ext_vector_type(8)));
typedef _Float16 f16x2 __attribute__((ext_vector_type(2)));
typedef short s16x4 __attribute__((ext_vector_type(4)));
typedef unsigned char u8;
typedef const unsigned short* cus;
typedef GAS unsigned gu32;
typedef GAS unsigned long long gu64;

constexpr int L_ = 2048, D_ = 512, N_ = 128, F_ = 1792;

constexpr int XP_BYTES = L_ * D_ * 2, W_BYTES = D_ * F_ * 2, H_BYTES = L_ * F_ * 2;
#define REUSE 0
constexpr int LB_BYTES = REUSE ? XP_BYTES : 3 * XP_BYTES;
constexpr int LB_X1 = 0;
constexpr int LB_XIN = XP_BYTES;
constexpr int LB_UIN = 2 * XP_BYTES;
constexpr int LB_PS1 = 3 * XP_BYTES + LB_BYTES;
constexpr int OFF_W1F = LB_PS1 + 262144;
constexpr int LB_H = LB_PS1 + LB_BYTES + 262144;
static_assert(OFF_W1F + W_BYTES <= LB_PS1 + LB_BYTES, "w1 fragments sit between the two partial-sum blocks");
constexpr int OFF_W2F = LB_H + 2 * H_BYTES;
constexpr int OFF_PS2 = OFF_W2F + W_BYTES;
#define FLS 64
constexpr int OFF_FLAG = OFF_PS2 + 262144;
constexpr int OFF_EPOCH = OFF_FLAG + 262144;
constexpr int WS_BYTES = OFF_EPOCH + 4096;
constexpr int OFF_XP1 = LB_X1, OFF_XP2 = LB_BYTES + LB_XIN, OFF_XP3 = LB_BYTES + LB_X1;

constexpr int LDS_W1 = 0, LDS_SCR = 114688, LDS_TOTAL = 163840;
constexpr int KC_STRIDE = 4160, KC_CH = 4 * KC_STRIDE;
constexpr int U_CH = (256 + 2048) * 2 + 64;
constexpr int CV_K = LDS_SCR, CV_U = CV_K + 2 * KC_CH, CV_EX = LDS_SCR, EX_TILE = 1152;
constexpr int CS_OFF = CV_U + 2 * U_CH;
static_assert(CS_OFF + 512 <= LDS_TOTAL && CV_EX + 32 * EX_TILE <= LDS_TOTAL, "lds");
constexpr int RD = 6, RING_W = RD * 2048;
constexpr int GS_ROW = 132;
constexpr int KG_KP = 81920, KG_RED = KG_KP + 3600 * 4, KG_GS = KG_RED + 1024, KG_ES = KG_GS + 2 * 41 * GS_ROW * 4;
static_assert(KG_GS <= LDS_SCR && KG_ES + 2 * 16 * GS_ROW * 4 <= LDS_TOTAL, "kgen lds");
#define G2W 16
constexpr int G2_WIN = G2W;

constexpr int RSRC_W3 = 0x00020000, AUX_SC1 = 16;
constexpr unsigned SPIN_CAP = 1u << 17;
constexpr unsigned POISON = 0xFFFFFFFFu;
#define RLX_AGENT __ATOMIC_RELAXED, __HIP_MEMORY_SCOPE_AGENT
#define SB() __builtin_amdgcn_sched_barrier(0)
#define WAITV0() asm volatile("s_waitcnt vmcnt(0)" ::: "memory")
#define LDS_BAR() do { asm volatile("s_waitcnt lgkmcnt(0)" ::: "memory"); __builtin_amdgcn_s_barrier(); asm volatile("" ::: "memory"); } while (0)
template <int N> __device__ __forceinline__ void waitv() { asm volatile("s_waitcnt vmcnt(%0)" ::"i"(N) : "memory"); }

__device__ __forceinline__ float bf_lo(unsigned u) { return __builtin_bit_cast(float, u << 16); }
__device__ __forceinline__ float bf_hi(unsigned u) { return __builtin_bit_cast(float, u & 0xFFFF0000u); }
__device__ __forceinline__ float bf1(unsigned short u) { return __builtin_bit_cast(float, (unsigned)u << 16); }
__device__ __forceinline__ unsigned pk_bf16(float a, float b) { bf16x2 v = {(__bf16)a, (__bf16)b}; return __builtin_bit_cast(unsigned, v); }
__device__ __forceinline__ unsigned pk_f16(float a, float b) { f16x2 v = {(_Float16)a, (_Float16)b}; return __builtin_bit_cast(unsigned, v); }
__device__ __forceinline__ float rbf(float a) { return (float)(__bf16)a; }
__device__ __forceinline__ unsigned san(unsigned u) { return min(u, POISON - 1u); }
__device__ __forceinline__ v4u san4(v4u v) { return v4u{san(v.x), san(v.y), san(v.z), san(v.w)}; }
__device__ __forceinline__ unsigned umax3(unsigned a, unsigned b, unsigned c) { return max(max(a, b), c); }
__device__ __forceinline__ unsigned umax4(unsigned m, v4u v) { const unsigned a = v.x, b = v.y, c = v.z, d = v.w; return umax3(umax3(m, a, b), c, d); }
__device__ __forceinline__ f32x4 mfma_bf(v4u a, v4u b, f32x4 c) {
  return __builtin_amdgcn_mfma_f32_16x16x32_bf16(__builtin_bit_cast(bf16x8, a), __builtin_bit_cast(bf16x8, b), c, 0, 0, 0);
}
__device__ __forceinline__ f32x4 mfma_h(v4u a, v4u b, f32x4 c) {
  return __builtin_amdgcn_mfma_f32_16x16x32_f16(__builtin_bit_cast(f16x8, a), __builtin_bit_cast(f16x8, b), c, 0, 0, 0);
}
__device__ __forceinline__ float dot2bf(unsigned a, unsigned b, float c) {
  return __builtin_amdgcn_fdot2_f32_bf16(__builtin_bit_cast(bf16x2, a), __builtin_bit_cast(bf16x2, b), c, false);
}
__device__ __forceinline__ f32x2 pkfma(f32x2 a, f32x2 b, f32x2 c) { return __builtin_elementwise_fma(a, b, c); }
__device__ __forceinline__ v2u tr_read(const LAS u8* p) { return __builtin_bit_cast(v2u, __builtin_amdgcn_ds_read_tr16_b64_v4i16((LAS s16x4*)p)); }
__device__ __forceinline__ void st128(v4u v, __amdgpu_buffer_rsrc_t rs, int off) { __builtin_amdgcn_raw_buffer_store_b128(v, rs, off, 0, AUX_SC1); }
__device__ __forceinline__ void st64(v2u v, __amdgpu_buffer_rsrc_t rs, int off) { __builtin_amdgcn_raw_buffer_store_b64(v, rs, off, 0, AUX_SC1); }
__device__ __forceinline__ void st32(unsigned v, __amdgpu_buffer_rsrc_t rs, int off) { __builtin_amdgcn_raw_buffer_store_b32(v, rs, off, 0, AUX_SC1); }
__device__ __forceinline__ v4u ld128(__amdgpu_buffer_rsrc_t rs, int voff, int soff) { return __builtin_amdgcn_raw_buffer_load_b128(rs, voff, soff, AUX_SC1); }

__device__ __forceinline__ int chi(int pos) { return 32 * (pos >> 5) + 2 * (pos & 15) + ((pos >> 4) & 1); }

template <int CTRL> __device__ __forceinline__ float dppf(float v) {
  return __builtin_bit_cast(float, __builtin_amdgcn_update_dpp(0, __builtin_bit_cast(int, v), CTRL, 0xF, 0xF, true));
}
template <int BANK> __device__ __forceinline__ float selbank(float a, float b) {
  return __builtin_bit_cast(float, __builtin_amdgcn_update_dpp(__builtin_bit_cast(int, a), __builtin_bit_cast(int, b), 0xE4, 0xF, BANK, false));
}
__device__ __forceinline__ float rowreduce16(const float (&v)[16], int c) {
  float a[8], b[4];
#pragma unroll
  for (int i = 0; i < 8; ++i) a[i] = selbank<0xC>(v[i] + dppf<0x128>(v[i]), v[i + 8] + dppf<0x128>(v[i + 8]));
#pragma unroll
  for (int i = 0; i < 4; ++i) b[i] = selbank<0xA>(a[i] + dppf<0x141>(a[i]), a[i + 4] + dppf<0x141>(a[i + 4]));
  const float b0 = b[0] + dppf<0x4E>(b[0]), b1 = b[1] + dppf<0x4E>(b[1]), b2 = b[2] + dppf<0x4E>(b[2]), b3 = b[3] + dppf<0x4E>(b[3]);
  const float c0 = (c & 2) ? b2 : b0, c1 = (c & 2) ? b3 : b1;
  const float d0 = c0 + dppf<0xB1>(c0), d1 = c1 + dppf<0xB1>(c1);
  return (c & 1) ? d1 : d0;
}
__device__ __forceinline__ float xor16_sum(float v) {
  const unsigned u = __builtin_bit_cast(unsigned, v);
  auto r = __builtin_amdgcn_permlane16_swap(u, u, false, false);
  const unsigned r0 = r[0], r1 = r[1];
  return __builtin_bit_cast(float, r0) + __builtin_bit_cast(float, r1);
}
__device__ __forceinline__ float xor32_sum(float v) {
  const unsigned u = __builtin_bit_cast(unsigned, v);
  auto r = __builtin_amdgcn_permlane32_swap(u, u, false, false);
  const unsigned r0 = r[0], r1 = r[1];
  return __builtin_bit_cast(float, r0) + __builtin_bit_cast(float, r1);
}
__device__ __forceinline__ float hsum(float v) { return xor32_sum(xor16_sum(v)); }
template <int N> __device__ __forceinline__ float rowbc(float v) { return dppf<0x150 + N>(v); }
__device__ __forceinline__ void mean_rstd(float tot, int c, float& m, float& r) {
  const float oth = dppf<0xB1>(tot);
  const float sv = (c & 1) ? oth : tot, qv = (c & 1) ? tot : oth;
  m = sv * (1.f / D_);
  r = __builtin_amdgcn_rsqf(fmaxf(qv * (1.f / D_) - m * m, 0.f) + 1e-5f);
}
__device__ __forceinline__ void norm8(const unsigned (&pr)[8], float m, float r, v4u& ua, v4u& ub) {
  const float mk[8] = {rowbc<0>(m), rowbc<2>(m), rowbc<4>(m), rowbc<6>(m), rowbc<8>(m), rowbc<10>(m), rowbc<12>(m), rowbc<14>(m)};
  const float rk[8] = {rowbc<0>(r), rowbc<2>(r), rowbc<4>(r), rowbc<6>(r), rowbc<8>(r), rowbc<10>(r), rowbc<12>(r), rowbc<14>(r)};
#pragma unroll
  for (int j = 0; j < 4; ++j) {
    ua[j] = san(pk_f16((bf_lo(pr[2 * j]) - mk[2 * j]) * rk[2 * j], (bf_lo(pr[2 * j + 1]) - mk[2 * j + 1]) * rk[2 * j + 1]));
    ub[j] = san(pk_f16((bf_hi(pr[2 * j]) - mk[2 * j]) * rk[2 * j], (bf_hi(pr[2 * j + 1]) - mk[2 * j + 1]) * rk[2 * j + 1]));
  }
}
__device__ __forceinline__ void tr8x2(const unsigned (&r)[8], v4u& o0, v4u& o1) {
#pragma unroll
  for (int j = 0; j < 4; ++j) {
    o0[j] = __builtin_amdgcn_perm(r[2 * j + 1], r[2 * j], 0x05040100u);
    o1[j] = __builtin_amdgcn_perm(r[2 * j + 1], r[2 * j], 0x07060302u);
  }
}
__device__ __forceinline__ float stats16(const unsigned (&pr)[8], int c) {
  float v[16];
#pragma unroll
  for (int k = 0; k < 8; ++k) { v[2 * k] = dot2bf(pr[k], 0x3F803F80u, 0.f); v[2 * k + 1] = dot2bf(pr[k], pr[k], 0.f); }
  return rowreduce16(v, c);
}
__device__ __forceinline__ unsigned mish2(f32x2 hl) {
  const f32x2 e = {__builtin_amdgcn_exp2f(hl.x), __builtin_amdgcn_exp2f(hl.y)};
  const f32x2 two = {2.f, 2.f};
  const f32x2 q = pkfma(e, e + two, two);
  const f32x2 rc = {__builtin_amdgcn_rcpf(q.x), __builtin_amdgcn_rcpf(q.y)};
  const f32x2 ra = pkfma(rc, f32x2{-1.38629436111989f, -1.38629436111989f}, f32x2{0.693147180559945f, 0.693147180559945f});
  const f32x2 a = hl * ra;
  return san(pk_bf16(a.x, a.y));
}
__device__ __forceinline__ f32x2 cmul(f32x2 a, f32x2 b) { return pkfma(a.yy, f32x2{-b.y, b.x}, a.xx * b); }
__device__ __forceinline__ f32x2 cexp_m(float ar, float ai, float m) {
  const float mag = __builtin_amdgcn_exp2f(ar * m * 1.44269504088896f);
  const float rev = __builtin_amdgcn_fractf(ai * m * 0.159154943091895f);
  return f32x2{mag * __builtin_amdgcn_cosf(rev), mag * __builtin_amdgcn_sinf(rev)};
}
__device__ __forceinline__ void wg_bar() { __builtin_amdgcn_fence(__ATOMIC_ACQUIRE, "wavefront"); LDS_BAR(); }

template <int I> struct IC { static constexpr int v = I; };
template <int I, int N, class Fn> __device__ __forceinline__ void static_for(Fn&& f) {
  if constexpr (I < N) { f(IC<I>{}); static_for<I + 1, N>(f); }
}
struct CvSched { int ga[108], gb[108], first[108], rd[108]; };
constexpr CvSched make_cvsched() {
  CvSched s{};
  int m = 0, rq = 12;
  for (int si = 0; si < 3; ++si)
    for (int d = 0; d < 8; ++d)
      for (int g2 = 0; g2 <= 2 * d; ++g2) {
        const int g = g2 >> 1;
        if (g + d >= 8) continue;
        s.ga[m] = (g2 & 1) ? d : g; s.gb[m] = (g2 & 1) ? g : d;
        s.first[m] = si == 0 && (g2 & 1) == 0 && (g == d || g + 1 == d);
        s.rd[m] = (rq < 24 * (si + 2) && (m < 36 || m % 3 != 2)) ? rq++ : -1;
        ++m;
      }
  return s;
}
constexpr CvSched CVS = make_cvsched();
constexpr int cvl_tile(int m) { int tl = 0; while ((tl + 1) * (tl + 2) / 2 <= m) ++tl; return tl; }
#define XTOUCH 0
#define HT0 99
#define HT1 99
#define POLL_H0 12
#define POLL_H1 16
#define WSD 2
#define NS_B 8
constexpr int NS_A = (10 + WSD) / WSD > 7 ? 7 : (10 + WSD) / WSD;
constexpr int WS_P0 = 9;
#define W2_CV 11
constexpr int W1_P0 = WS_P0 < 20 ? WS_P0 : 20, W2_PRE = WS_P0 - W1_P0;
#define NPK 3
#define CV_D1 4
#define CV_D2 5
constexpr int CV_M2 = CV_D1 * (28 - W1_P0) + 4;
constexpr int cv_w2_before(int m) { int n = 0; for (int i = CV_M2; i < m; ++i) n += (i - CV_M2) % CV_D2 == 0 && (i - CV_M2) / CV_D2 < W2_CV; return n; }
constexpr int BQ_M = CV_M2 + 1;
constexpr int W2_P0 = W2_PRE + W2_CV;
#define CHK_K 9
#define PSK 8
#define FIN_K 13
constexpr int PS_K = PSK;
constexpr int cvl_free(int m) { int n = 0; for (int i = 0; i < m; ++i) n += !((i >= 9 && i <= 29 && (i - 9) % 4 == 0) || i == 35); return n; }
#define W2_CS 8
constexpr int ST_K = 3;
constexpr bool g1_gap0_busy(int k) { return k == ST_K || (k >= PS_K && k < PS_K + 4); }
constexpr int g1_w2n(int ks, int q) {
  int n = W2_P0 + W2_CS;
  for (int k = 0; k < 16; ++k)
    for (int g = 0; g < 8; ++g) {
      const bool has = n < 56 && ((g == 0 && k >= 1 && !g1_gap0_busy(k)) || ((g == 6 || g == 7) && k >= 10));
      if (k == ks && g == q) return has ? n : -1;
      if (has) ++n;
    }
  return n;
}
constexpr int g1_wait(int L, int ks, int gq, int pc) {
  int pos[18] = {}, n = 0;
  const int order[6] = {0, 1, 16, 2, 3, 4};
  for (int k = 0; k < 6; ++k) { n += 2; pos[order[k]] = n; }
  for (int k = 0; k <= ks; ++k) {
    if (k == 1) { n += 2; pos[5] = n; }
    n += (g1_gap0_busy(k) || (L == 0 && g1_w2n(k, 0) >= 0)) ? 1 : 0;
    if (k == PS_K + 3) pos[17] = n;
    if (k == ks && gq < 7) break;
    if (k + 6 < 16) { n += 2; pos[k + 6] = n; } else if (L == 0) n += (g1_w2n(k, 6) >= 0) + (g1_w2n(k, 7) >= 0);
  }
  return n - pos[pc];
}
constexpr int g1_slots(int L, int ks, int q) {
  if (ks == 15) return (L == 0 && (q == 0 || q == 6 || q == 7)) ? 0 : 2;
  if (q < 2) return 0;
  if (q == 6 || q == 7) return (ks < 10 || L == 0) ? 0 : 2;
  if (q == 13 || q == 3 || q == 5) return 0;
  if (q == 4 && ks >= ST_K && ks < 10) return 0;
  if (ks < CHK_K && q >= 8 && q <= 10) return 0;
  return 1;
}
constexpr int g1_slot0(int L, int k0, int ks, int q) { int n = 0; for (int m = k0 * 14; m < ks * 14 + q; ++m) n += g1_slots(L, m / 14, m % 14); return n; }
constexpr int W2_AFTER_LOOP = g1_w2n(99, 0);

constexpr int g2k(int k) { return k < 12 ? 7 * ((k >> 1) & 1) + 3 * (k & 1) + (k >> 2) : 7 * (k - 12) + 6; }
#define G1_DELAY 10
#define EARLY_M 6
#define G2C 4
#define FLAG_H 3
#define HSHARE 0
#define TLBW 0
#define NOCHK 0
#define NOWAIT 0
#define EARLYIDX 0
#define PO_0 14
#define PO_D 14
#define G0_DELAY 3
#define U_RS 0
#define X_RS 3
#define NEARPF 1
#define U_AUX 16
#define U_ACK 1
#define P0_0 15
#define P0_D 16
#define POLL_Z 0
#define POLL_LATE 0
#define EARLYPO 1
#define PREX 0
#define TABM 2
#define PFM 0
#define B1PF 1
#define STAG 0
#define APRE 0
#define WT_H -1
#define WLAST 0
#define U_DELAY 3

constexpr int g2_lrow(int n) { return n < 32 ? n >> 4 : 2 + (n - 32) / 12; }
constexpr int g2_idx(int n) { return n < 32 ? n & 15 : (n - 32) % 12; }
constexpr int g2_ks(int n) { return g2_idx(n) < 12 ? g2k(g2_idx(n)) : g2_idx(n) < 14 ? 6 : 13; }
constexpr int g2_imm(int n) { return n >= 32 ? 1024 : g2_idx(n) >= 12 ? (n & 1) * 1024 : 0; }
constexpr int g2_arow(int n) { return g2_lrow(n) + (g2_idx(n) >= 12 && (n & 1) ? 2 : 0); }
constexpr bool g2_first(int n) { return n < 32 && (g2_idx(n) == 0 || g2_idx(n) == 13); }
#define EH 1
constexpr int g2_eh(int m) {
  int c = 0;
  for (int i = 45 * 4; i < m; ++i) {
    const int n = i >> 2, j = i & 3, k = n - 44;
    const bool wr = j == 1 && (k & 1) == 1 && k >= 3 && k <= 9;
    c += wr ? 0 : j == 0 ? (n < 52 ? 1 : 2) : j == 2 ? (n < 52 ? 0 : 2) : j == 3 ? ((n + 1) % 4 == 0 && n < 55 ? 0 : 2) : 2;
  }
  return c;
}
constexpr int XHI = 32 * 32768;
constexpr int DROP = 0x7F000000;

constexpr int bf16_round_pos(int v) {
  int sh = 0;
  while ((v >> sh) >= 256) ++sh;
  if (sh == 0) return v;
  return ((v + (1 << (sh - 1)) - 1 + ((v >> sh) & 1)) >> sh) << sh;
}
constexpr int cv_po_between(int a, int b) { int n = 0; for (int q = 0; q < 7 - NPK; ++q) n += P0_0 + q * P0_D >= a && P0_0 + q * P0_D < b; return n; }
constexpr int cv_w1_between(int a, int b) { int n = 0; for (int m = a; m < b; ++m) n += m % CV_D1 == 2 && W1_P0 + m / CV_D1 < 28; return n; }
struct ThrTab { int v[12][256][4]; };
template <bool RT> __device__ __forceinline__ constexpr void thr_fill(int t, int w, int (&e)[48]) {
    const int lane = t & 63,  c = lane & 15, h = lane >> 4, gsel = w >> 1, dsel = w & 1, cc = (3 - c) & 3;
    const int t4 = t >> 4, hi7 = t4 >= 7, u7 = t4 - 7 * hi7;
    e[0] = OFF_W1F + (t & 15) * 16 + (u7 < 6 ? (2 * hi7 + (u7 >= 3)) * 112 + 2 * (u7 - 3 * (u7 >= 3)) : 2 * hi7 * 112 + 6) * 1024;
    e[1] = e[0] + (u7 < 6 ? 1024 : 114688);
    if (t >= 224) e[0] = e[1] = DROP;
    e[2] = OFF_W2F + (((t >> 5) * 56 * 4 + 2 * ((t >> 4) & 1)) * 64 + (t & 15)) * 16;
    e[3] = w * 2048 + c * 64 + h * 16;
    e[4] = (t >> 3) * 32768 + (t & 7) * 16;
    e[5] = gsel * 32768 + (32 * dsel + c) * 64 + h * 16;
    e[6] = h == 3 ? DROP : (h == 2 ? OFF_PS2 + (gsel * 16 + dsel) * 256 : h * LB_BYTES + LB_PS1 + w * 4096) + c * 16;
    e[7] = LB_H + (gsel * 56 * 4 + 2 * dsel) * 1024 + lane * 16;
    e[8] = CV_K + 16 * t;
    const int tz = t < 96 ? t : (t & 63);
    e[9] = tz < 32 ? CV_K + (tz >> 2) * KC_STRIDE + 4096 + (tz & 3) * 16 : CV_U + ((tz - 32) >> 5) * U_CH + (tz & 31) * 16;
    e[10] = CV_K + gsel * KC_CH + cc * KC_STRIDE + 2 * (15 - c + 8 * h - cc) + 64 * (4 - 4 * dsel);
    e[11] = e[10] + 8;
    e[12] = CV_U + gsel * U_CH + 480 + 32 * c + 16 * h - 64 * (4 * dsel + 3);
    e[13] = CV_EX + (w * 8) * EX_TILE + (lane + 8 * (h >> 1)) * 16;
    e[14] = CV_EX + (((t >> 2) & 1) * 16 + w) * EX_TILE + (((lane >> 3) * 2 + ((t >> 1) & 1)) + 40 * (t & 1)) * 16;
    e[15] = CV_U + ((t >> 2) & 1) * U_CH + 512 + ((t >> 3) * 4 + (t & 3)) * 16;
    e[16] = LDS_SCR + w * RING_W + (h & 1) * 1024 + (8 * (h >> 1) + (c >> 2)) * 64 + (c & 3) * 16 + 8 * (h & 1);
    e[17] = e[16] + 8 - 16 * (h & 1);
    e[18] = lane * 16 + (1 - gsel) * 2048;
    e[19] = lane * 16 + gsel * 2048;
    e[20] = LDS_SCR + ((w ^ 1) * 3 + 0) * 4096 + lane * 16;
    e[21] = LDS_SCR + ((w ^ 2) * 3 + 1) * 4096 + lane * 16;
    e[22] = LDS_SCR + ((w ^ 3) * 3 + 2) * 4096 + lane * 16;
    e[23] = LDS_SCR + w * 12288 + lane * 16;
#pragma unroll
    for (int z = 0; z < 8; ++z) {
      const int reg = z & 3;
      const int o0 = 128 * (h >> 1) + 4 * (h & 1) + reg;
      const int o1 = h < 2 ? 256 + 128 * h + 2 * reg : 256 * h + 128 * (reg >> 1) + 4 * (reg & 1);
      const int o2 = 1024 + 512 * h + 128 * reg;
      const int oo = (dsel ? o2 : z < 4 ? o0 : o1) + 8 * c;
      e[24 + z] = KG_KP + (oo + (oo >> 3)) * 4 + 2 * gsel;
    }
#pragma unroll
    for (int i = 0; i < 11; ++i) {
      const int lag = 2047 - (8 * t + i), pp = RT ? (int)(float)(__bf16)(float)lag : lag < 0 ? 0 : bf16_round_pos(lag);
      e[32 + i] = KG_KP + (lag < 0 ? 8 : pp + (pp >> 3)) * 4;
    }
    e[47] = KG_GS + (gsel * 41 * GS_ROW + (t & 127)) * 4;
    e[44] = KG_ES + (gsel * 16 * GS_ROW + (t & 127)) * 4;
    e[45] = KG_GS + ((gsel * 41 + dsel * 32 + c) * GS_ROW + 4 * h) * 4;
    e[46] = e[45] + (1 - dsel) * 16 * GS_ROW * 4;
    e[43] = KG_ES + ((gsel * 16 + c) * GS_ROW + 4 * h) * 4;
}
constexpr ThrTab make_thrtab() {
  ThrTab T{};
  for (int t = 0; t < 256; ++t) {
    int e[48] = {};
    thr_fill<false>(t, t >> 6, e);
    for (int k = 0; k < 48; ++k) T.v[k >> 2][t][k & 3] = e[k];
  }
  return T;
}
__device__ const ThrTab THR = make_thrtab();
static_assert(bf16_round_pos(257) == 256 && bf16_round_pos(259) == 260 && bf16_round_pos(2044) == 2048 && bf16_round_pos(2036) == 2032, "rne");

struct Params {
  cus x0, lar, aim, cr, ci, ldt, dsk, w1, b1, w2;
  u8* ws;
  unsigned* out;
};

#define DI __device__ __forceinline__
DI void MFMA_V(f32x4& acc, v4u a, v4u b) { asm volatile("v_mfma_f32_16x16x32_bf16 %0, %1, %2, %0" : "+v"(acc) : "v"(a), "v"(b)); }
DI void MFMA_V0(f32x4& acc, v4u a, v4u b) { asm volatile("v_mfma_f32_16x16x32_bf16 %0, %1, %2, 0" : "=&v"(acc) : "v"(a), "v"(b)); }
DI void MFMA_A(f32x4& acc, v4u a, v4u b) { asm volatile("v_mfma_f32_16x16x32_bf16 %0, %1, %2, %0" : "+v"(acc) : "v"(a), "a"(b)); }
DI void MFMA_A0(f32x4& acc, v4u a, v4u b) { asm volatile("v_mfma_f32_16x16x32_bf16 %0, %1, %2, 0" : "=&v"(acc) : "v"(a), "a"(b)); }
DI void MFMA_H(f32x4& acc, v4u a, v4u b) { asm volatile("v_mfma_f32_16x16x32_f16 %0, %1, %2, %0" : "+v"(acc) : "v"(a), "v"(b)); }
DI void MFMA_H0(f32x4& acc, v4u a, v4u b) { asm volatile("v_mfma_f32_16x16x32_f16 %0, %1, %2, 0" : "=&v"(acc) : "v"(a), "v"(b)); }
DI float i_add(float a, float b) { float d; asm volatile("v_add_f32 %0, %1, %2" : "=v"(d) : "v"(a), "v"(b)); return d; }
DI float i_mov(float a) { float d; asm volatile("v_mov_b32 %0, %1" : "=v"(d) : "v"(a)); return d; }
DI void i_sw16(float& a, float& b) { asm volatile("v_permlane16_swap_b32 %0, %1" : "+v"(a), "+v"(b)); }
DI void i_sw32(float& a, float& b) { asm volatile("v_permlane32_swap_b32 %0, %1" : "+v"(a), "+v"(b)); }
DI float i_sel(float a, float b, unsigned long long m) { float d; asm volatile("v_cndmask_b32 %0, %1, %2, %3" : "=v"(d) : "v"(a), "v"(b), "s"(m)); return d; }
DI float i_cvt_lo(unsigned u) { float d; asm volatile("v_cvt_f32_f16 %0, %1" : "=v"(d) : "v"(u)); return d; }
DI float i_cvt_hi(unsigned u) { float d; asm volatile("v_cvt_f32_f16_sdwa %0, %1 dst_sel:DWORD dst_unused:UNUSED_PAD src0_sel:WORD_1" : "=v"(d) : "v"(u)); return d; }
DI void swap_halves(v4u& e, v4u& o) {
  unsigned a = e.z, b = e.w, c = o.x, d = o.y;
  asm volatile("v_swap_b32 %0, %1" : "+v"(a), "+v"(c));
  asm volatile("v_swap_b32 %0, %1" : "+v"(b), "+v"(d));
  e.z = a; e.w = b; o.x = c; o.y = d;
}
DI void stagger(int w) {
  if (w & 1) asm volatile("s_nop 3");
  if (w & 2) asm volatile("s_nop 8");
}
template <class T> DI void PINV(T& x) { asm volatile("" : "+v"(x)); }
template <class T> DI void DEFV(T& x) { asm volatile("" : "=v"(x)); }
template <class T> DI void PINS(T& x) { asm volatile("" : "+s"(x) :: "memory"); }
template <int IMM> DI void set_m0(unsigned base) { asm volatile("s_add_u32 m0, %0, %1" ::"s"(base), "i"(IMM) : "memory", "scc"); }
DI void set_m0r(unsigned v) { asm volatile("s_mov_b32 m0, %0\n\ts_nop 4" ::"s"(v) : "memory"); }
template <int OFF> DI void dma16(int voff, __amdgpu_buffer_rsrc_t rs, int soff) {
  asm volatile("buffer_load_dwordx4 %0, %1, %2 offen offset:%3 sc1 lds" ::"v"(voff), "s"(rs), "s"(soff), "i"(OFF) : "memory");
}
#define WNT ""
template <int OFF> DI void dma16w(int voff, __amdgpu_buffer_rsrc_t rs, int soff) {
  asm volatile("buffer_load_dwordx4 %0, %1, %2 offen offset:%3 sc1 " WNT " lds" ::"v"(voff), "s"(rs), "s"(soff), "i"(OFF) : "memory");
}
template <int OFF> DI void ld128_a(v4u& dst, int voff, __amdgpu_buffer_rsrc_t rs, int soff) {
  asm volatile("buffer_load_dwordx4 %0, %1, %2, %3 offen offset:%4 sc1 " WNT : "=a"(dst) : "v"(voff), "s"(rs), "s"(soff), "i"(OFF) : "memory");
}
DI void ld128_p(v4u& dst, int voff, __amdgpu_buffer_rsrc_t rs, int soff) {
  asm volatile("buffer_load_dwordx4 %0, %1, %2, %3 offen" : "=v"(dst) : "v"(voff), "s"(rs), "s"(soff) : "memory");
}
template <int OFF> DI void ld128_v(v4u& dst, int voff, __amdgpu_buffer_rsrc_t rs, int soff) {
  asm volatile("buffer_load_dwordx4 %0, %1, %2, %3 offen offset:%4 sc1" : "=v"(dst) : "v"(voff), "s"(rs), "s"(soff), "i"(OFF) : "memory");
}
template <int ID> DI void slow_begin() { asm volatile(".subsection 1\nLslow%c0:\n\ts_waitcnt vmcnt(0) lgkmcnt(0)\n\ts_sleep %1\n\t.subsection 0" ::"i"(ID), "i"(X_RS)); }
template <int SOFF, int OFF> DI void slow_ld(v4u dst, int voff, __amdgpu_buffer_rsrc_t rs, int base) {
  asm volatile(".subsection 1\n\ts_add_u32 vcc_lo, %3, %4\n\tbuffer_load_dwordx4 %0, %1, %2, vcc_lo offen offset:%5 sc1\n\t.subsection 0"
               ::"v"(dst), "v"(voff), "s"(rs), "s"(base), "i"(SOFF), "i"(OFF));
}
template <int SOFF, int MOFF> DI void slow_dma(int voff, __amdgpu_buffer_rsrc_t rs, int base, unsigned ring) {
  asm volatile(".subsection 1\n\ts_add_u32 m0, %3, %5\n\ts_add_u32 vcc_lo, %2, %4\n\tbuffer_load_dwordx4 %0, %1, vcc_lo offen sc1 lds\n\t"
               "buffer_load_dwordx4 %0, %1, vcc_lo offen offset:1024 sc1 lds\n\t.subsection 0" ::"v"(voff), "s"(rs), "s"(base), "s"(ring), "i"(SOFF), "i"(MOFF));
}
template <int ID> DI void check_h(unsigned& vm, unsigned& tries, v4u a0, v4u a1, v4u a2, v4u a3) {
  const unsigned b0 = a0.x, b1 = a0.y, b2 = a0.z, b3 = a0.w, b4 = a1.x, b5 = a1.y, b6 = a1.z, b7 = a1.w;
  const unsigned c0 = a2.x, c1 = a2.y, c2 = a2.z, c3 = a2.w, c4 = a3.x, c5 = a3.y, c6 = a3.z, c7 = a3.w;
  asm volatile(".subsection 1\n\ts_waitcnt vmcnt(0)\n\tv_max3_u32 %0, %4, %5, %6\n\tv_max3_u32 %0, %0, %7, %8\n\tv_max3_u32 %0, %0, %9, %10\n\tv_max3_u32 %0, %0, %11, %12\n\t"
               "v_max3_u32 %0, %0, %13, %14\n\tv_max3_u32 %0, %0, %15, %16\n\tv_max3_u32 %0, %0, %17, %18\n\tv_max_u32 %0, %0, %19\n\t"
               "s_add_u32 %1, %1, 1\n\ts_cmp_lt_u32 %1, %3\n\ts_cbranch_scc1 Lchk%c2\n\ts_branch Lok%c2\n\t.subsection 0\n"
               "Lchk%c2:\n\tv_cmp_eq_u32 vcc, -1, %0\n\ts_cbranch_vccnz Lslow%c2\nLok%c2:"
               : "+v"(vm), "+s"(tries) : "i"(ID), "i"(SPIN_CAP), "v"(b0), "v"(b1), "v"(b2), "v"(b3), "v"(b4), "v"(b5), "v"(b6), "v"(b7),
                 "v"(c0), "v"(c1), "v"(c2), "v"(c3), "v"(c4), "v"(c5), "v"(c6), "v"(c7) : "vcc", "scc", "memory");
}
DI unsigned long long late_x(unsigned vm, unsigned thr) { unsigned long long m; asm volatile("v_cmp_lt_u32 %0, %1, %2" : "=s"(m) : "s"(thr), "v"(vm)); return m; }
template <int ID, int OFF> DI void check_x(unsigned long long lm, unsigned& tries, unsigned& thr, v2u l0, v2u h0, v2u l1, v2u h1, const LAS u8* p0, const LAS u8* p1) {
  const unsigned b0 = l0.x, b1 = l0.y, b2 = h0.x, b3 = h0.y;
  unsigned vm;
  asm volatile(".subsection 1\n\ts_waitcnt vmcnt(0)\n\tds_read_b64_tr_b16 %4, %8 offset:%10\n\tds_read_b64_tr_b16 %5, %8 offset:%11\n\t"
               "ds_read_b64_tr_b16 %6, %9 offset:%10\n\tds_read_b64_tr_b16 %7, %9 offset:%11\n\ts_add_u32 %1, %1, 1\n\ts_cmp_lt_u32 %1, 64\n\t"
               "s_cselect_b32 %2, %2, -1\n\ts_waitcnt lgkmcnt(0)\n\tv_max3_u32 %0, %12, %13, %14\n\tv_max_u32 %0, %0, %15\n\t"
               "v_cmp_lt_u32 vcc, %2, %0\n\ts_cbranch_vccnz Lslow%c3\n\ts_branch Lok%c3\n\t.subsection 0\n\t"
               "s_cmp_lg_u64 %16, 0\n\ts_cbranch_scc1 Lslow%c3\nLok%c3:"
               : "=&v"(vm), "+s"(tries), "+s"(thr) : "i"(ID), "v"(l0), "v"(h0), "v"(l1), "v"(h1), "v"(p0), "v"(p1), "i"(OFF), "i"(OFF + 256),
                 "v"(b0), "v"(b1), "v"(b2), "v"(b3), "s"(lm) : "vcc", "scc", "memory");
}
DI void ld128x2_v(v4u& d0, v4u& d1, int voff, __amdgpu_buffer_rsrc_t rs, int soff) {
  asm volatile("buffer_load_dwordx4 %0, %2, %3, %4 offen sc1\n\tbuffer_load_dwordx4 %1, %2, %3, %5 offen sc1"
               : "=&v"(d0), "=&v"(d1) : "v"(voff), "s"(rs), "s"(soff), "s"(soff + XHI) : "memory");
}
DI void touch1(unsigned& d, int voff, __amdgpu_buffer_rsrc_t rs, int s0) {
  asm volatile("buffer_load_dword %0, %1, %2, %3 offen sc1" : "+v"(d) : "v"(voff), "s"(rs), "s"(s0) : "memory");
}
DI void touch2(unsigned& d, int voff, __amdgpu_buffer_rsrc_t rs, int s0, int s1) {
  asm volatile("buffer_load_dword %0, %1, %2, %3 offen sc1\n\tbuffer_load_dword %0, %1, %2, %4 offen sc1" : "+v"(d) : "v"(voff), "s"(rs), "s"(s0), "s"(s1) : "memory");
}
__device__ __forceinline__ float hmax(float v) {
  unsigned u = __builtin_bit_cast(unsigned, v);
  auto r = __builtin_amdgcn_permlane16_swap(u, u, false, false);
  unsigned r0 = r[0], r1 = r[1];
  u = max(r0, r1);
  auto q = __builtin_amdgcn_permlane32_swap(u, u, false, false);
  r0 = q[0]; r1 = q[1];
  return __builtin_bit_cast(float, max(r0, r1));
}
template <int CTRL> __device__ __forceinline__ unsigned dppu(unsigned v) { return (unsigned)__builtin_amdgcn_update_dpp(0, (int)v, CTRL, 0xF, 0xF, true); }
__device__ __forceinline__ unsigned ld32(__amdgpu_buffer_rsrc_t rs, int voff, int soff) { return __builtin_amdgcn_raw_buffer_load_b32(rs, voff, soff, AUX_SC1); }
__device__ __forceinline__ void hint_add(__amdgpu_buffer_rsrc_t rs, int voff, int soff) {
  (void)__builtin_amdgcn_raw_ptr_buffer_atomic_add_i32(1, rs, voff, soff, AUX_SC1);
}

__global__ void __launch_bounds__(256, 1) k_main(Params p) {
  extern __shared__ __attribute__((aligned(16))) u8 smem[];
  LAS u8* lds = (LAS u8*)smem;
  const int t = threadIdx.x, b = blockIdx.x, lane = t & 63, w = __builtin_amdgcn_readfirstlane(t >> 6);
  const int c = lane & 15, h = lane >> 4, xcd = b & 7, idx = b >> 3, pair = xcd * 32 + idx;
  const int rb = 4 * (xcd >> 1) + (idx & 3), cb = 8 * (xcd & 1) + (idx >> 2);
  const int tb = 2 * rb + (xcd & 1), db = idx >> 2;
  const int gsel = w >> 1, dsel = w & 1, g1 = 4 * rb + w, g2 = 2 * tb + gsel;
  const unsigned lds0 = (unsigned)(uintptr_t)lds;
  const __amdgpu_buffer_rsrc_t r0 = __builtin_amdgcn_make_buffer_rsrc((void*)p.ws, 0, WS_BYTES, RSRC_W3);
  v4u W2[14][4];
  unsigned kv[11];
  f32x4 csa[7];
  f32x2 ksd;
  v4u PZ = {POISON, POISON, POISON, POISON};

  const int lg = xcd * 8 + (idx >> 2), lq = idx & 3;
  unsigned codepf, tabpf, nearpf, b1pf = 0;
  unsigned short lar_, aim_, cr_, ci_, cr2_, ci2_, ldt_, dsk_;
  v2u ep;
  v4u tv[12];
  int vl = lane * 16, xio, xro;
  const __amdgpu_buffer_rsrc_t rt = __builtin_amdgcn_make_buffer_rsrc((void*)&THR, 0, sizeof(ThrTab), RSRC_W3);
#define LP(k) ((LAS u8*)(uintptr_t)(unsigned)tv[(k) >> 2][(k) & 3])
#define TI(k) ((int)tv[(k) >> 2][(k) & 3])
  auto tget = [&](auto K) {
    if constexpr (TABM == 1 || (TABM == 2 && K.v >= 2 && K.v < 11)) tv[K.v] = __builtin_amdgcn_raw_buffer_load_b128(rt, t * 16, K.v * 4096, 0);
    else { int e[48]; thr_fill<true>(t, w, e); tv[K.v] = v4u{(unsigned)e[4 * K.v], (unsigned)e[4 * K.v + 1], (unsigned)e[4 * K.v + 2], (unsigned)e[4 * K.v + 3]}; PINV(tv[K.v]); }
  };
  unsigned a1[8], a2[8];
  auto wpub = [&] {
    v4u o0, o1;
    const int wb1 = (b & 3) * 458752 + (b >> 4) * 7168 + ((b >> 2) & 3) * 256;
    const int wd0 = TI(0) + wb1, wd1 = TI(1) + wb1, wd2 = TI(2) + (b < 224 ? (b >> 2) * 4096 + (b & 3) * 256 : 0x40000000);
    tr8x2(a2, o0, o1);
    st128(o0, r0, wd2); st128(o1, r0, wd2 + 1024);
    tr8x2(a1, o0, o1);
    st128(o0, r0, wd0); st128(o1, r0, wd1);
  };
  {
    v4u xs_[8];
    unsigned xr[8];
    unsigned long long xp, pc;
    unsigned len;
    asm volatile("s_load_dwordx2 %0, %4, 0x0\n\ts_getpc_b64 %1\nk_code_begin:\n\tv_lshlrev_b32 %3, 7, %5\n\tv_and_b32 %3, 0x1f80, %3\n\t"
                 "global_load_dword %3, %3, %1\n\ts_mov_b32 %2, k_code_end-k_code_begin"
                 : "=&s"(xp), "=&s"(pc), "=&s"(len), "=&v"(nearpf) : "s"(__builtin_amdgcn_kernarg_segment_ptr()), "v"(t) : "memory");
    SB();
    int xvo = (t >> 3) * 1024 + (t & 7) * 16, xso = lg * 32768;
    asm volatile("s_waitcnt lgkmcnt(0)" : "+s"(xp), "+v"(xvo), "+s"(xso));
    const __amdgpu_buffer_rsrc_t rx = __builtin_amdgcn_make_buffer_rsrc((void*)xp, 0, XP_BYTES, RSRC_W3);
#pragma unroll
    for (int i = 0; i < 8; ++i) xs_[i] = __builtin_amdgcn_raw_buffer_load_b128(rx, xvo + i * 128, xso, 0);
    SB();
    {
      const __amdgpu_buffer_rsrc_t ri = __builtin_amdgcn_make_buffer_rsrc((void*)pc, 0, len, RSRC_W3);
      codepf = __builtin_amdgcn_raw_buffer_load_b32(ri, (t & 15) * 4096, idx * 128, 0);
      tabpf = __builtin_amdgcn_raw_buffer_load_b32(rt, (t & 15) * 4096, idx * 128, 0);
    }
    SB();
    const __amdgpu_buffer_rsrc_t ra = __builtin_amdgcn_make_buffer_rsrc((void*)p.w1, 0, W_BYTES, RSRC_W3);
    const __amdgpu_buffer_rsrc_t rc = __builtin_amdgcn_make_buffer_rsrc((void*)p.w2, 0, W_BYTES, RSRC_W3);
#pragma unroll
    for (int j = 0; j < 8; ++j)
      a2[j] = __builtin_amdgcn_raw_buffer_load_b32(rc, t * 4 + ((2 * (j & 3) + (j >> 2)) & 3) * 1024, b * 8192 + ((j & 3) >> 1) * 4096, 0);
    const int dg = b >> 2;
#pragma unroll
    for (int j = 0; j < 8; ++j)
      a1[j] = __builtin_amdgcn_raw_buffer_load_b32(ra, t * 4, (32 * (dg >> 2) + 16 * ((dg >> 1) & 1) + 2 * j + (dg & 1)) * (F_ * 2) + 896 * (b & 3), 0);
#pragma unroll
    for (int k = 0; k < 8; ++k)
      xr[k] = __builtin_amdgcn_raw_buffer_load_b32(rx, h * 8192 + c * 4 + (k & 3) * 1024, lg * 32768 + (64 * lq + 16 * w) * 4 + (k >> 2) * 4096, 0);
    {
      const int dch = chi(2 * pair + gsel), po = (t & 127) * 2;
#define PRM(ptr, bytes, vo, so) (unsigned short)__builtin_amdgcn_raw_buffer_load_b16(__builtin_amdgcn_make_buffer_rsrc((void*)(ptr), 0, bytes, RSRC_W3), vo, so, 0)
      lar_ = PRM(p.lar, D_ * N_ * 2, po, dch * 256); aim_ = PRM(p.aim, D_ * N_ * 2, po, dch * 256);
      cr_ = PRM(p.cr, D_ * N_ * 2, po, dch * 256); ci_ = PRM(p.ci, D_ * N_ * 2, po, dch * 256);
      cr2_ = PRM(p.cr, D_ * N_ * 2, po ^ 128, dch * 256); ci2_ = PRM(p.ci, D_ * N_ * 2, po ^ 128, dch * 256);
      ldt_ = PRM(p.ldt, D_ * 2, 0, dch * 2); dsk_ = PRM(p.dsk, D_ * 2, 0, dch * 2);
      ep = __builtin_amdgcn_raw_buffer_load_b64(r0, 0, OFF_EPOCH, AUX_SC1);
      if (B1PF) b1pf = PRM(p.b1, F_ * 2, (lane & 31) * 128, 0);
    }
    SB();
    tget(IC<0>{}); tget(IC<1>{});
    tget(IC<11>{});
    SB();
    xio = (t >> 3) * 32768 + (t & 7) * 16 + pair * 128;
    PINV(xio); PINV(PZ);
    if (EARLYPO) { st128(PZ, r0, xio); st128(PZ, r0, xio + XHI); }
    SB();
    SB();
    float s = 0.f, q = 0.f, s2 = 0.f, q2 = 0.f;
#pragma unroll
    for (int i = 0; i < 8; ++i) {
      const unsigned u0 = xs_[i].x, u1 = xs_[i].y, u2 = xs_[i].z, u3 = xs_[i].w;
      s = dot2bf(u0, 0x3F803F80u, s); q = dot2bf(u0, u0, q); s2 = dot2bf(u1, 0x3F803F80u, s2); q2 = dot2bf(u1, u1, q2);
      s = dot2bf(u2, 0x3F803F80u, s); q = dot2bf(u2, u2, q); s2 = dot2bf(u3, 0x3F803F80u, s2); q2 = dot2bf(u3, u3, q2);
    }
    s += s2; q += q2;
    s += dppf<0xB1>(s); q += dppf<0xB1>(q); s += dppf<0x4E>(s); q += dppf<0x4E>(q); s += dppf<0x141>(s); q += dppf<0x141>(q);
    const float m = s * (1.f / D_), r = __builtin_amdgcn_rsqf(fmaxf(q * (1.f / D_) - m * m, 0.f) + 1e-5f);
    LAS float* red = (LAS float*)(lds + KG_RED);
    if ((t & 7) == 0) { red[t >> 3] = r; red[32 + (t >> 3)] = -m * r; }
    const int pso_ = TI(6) + (h == 2 ? (tb * 32 + 2 * db) * 256 : (rb * 64 + cb) * 256);
    xro = TI(5) + tb * 65536 + db * 4096;
    PINV(xio); PINV(xro); PINV(vl); PINV(PZ);
    v4u o0, o1;
    if (WT_H < 0) wpub();
    tr8x2(xr, o0, o1);
    LDS_BAR();
    const int lo = lg * 32768 + lq * 8192 + TI(3);
    const LAS f32x4* rr = (const LAS f32x4*)(lds + KG_RED + h * 32);
    const f32x4 ra4 = rr[0], rb4 = rr[1], ma = rr[8], mb = rr[9];
    st128(o0, r0, LB_XIN + lo); st128(o1, r0, LB_XIN + lo + 1024);
    const f32x2 rk[4] = {ra4.xy, ra4.zw, rb4.xy, rb4.zw}, mk[4] = {ma.xy, ma.zw, mb.xy, mb.zw};
    v4u u0, u1;
#pragma unroll
    for (int j = 0; j < 4; ++j) {
      const unsigned x0 = o0[j], x1 = o1[j];
      const f32x2 y0 = pkfma(f32x2{bf_lo(x0), bf_hi(x0)}, rk[j], mk[j]), y1 = pkfma(f32x2{bf_lo(x1), bf_hi(x1)}, rk[j], mk[j]);
      u0[j] = pk_f16(y0.x, y0.y); u1[j] = pk_f16(y1.x, y1.y);
    }
    st128(u0, r0, LB_UIN + lo); st128(u1, r0, LB_UIN + lo + 1024);
    st128(PZ, r0, pso_);
    if (!EARLYPO) { st128(PZ, r0, xio); st128(PZ, r0, xio + XHI); }
    SB();
  }
  SB();
  int hb = TI(7) + (rb * 448 + (7 * (cb >> 1) + 3 * (cb & 1)) * 4) * 1024;
  int hodd = TI(7) + (rb * 448 + (cb & 1) + (7 * (cb >> 1) + 6) * 4) * 1024;
  const int sw = (pair >> 3) & 1;
  const LAS u8 *pw0 = (const LAS u8*)(uintptr_t)(unsigned)vl, *pw1 = pw0 + 57344;
  int h32 = h * 32, h16 = h * 16;
  PINV(h16); PINV(h32); PINV(hb); PINV(hodd); PINV(pw1);
  SB();
  const v2u tg = __builtin_bit_cast(v2u, __builtin_bit_cast(unsigned long long, ep) + 1ull);
  const int w2a = OFF_W2F + (db * 56 + 14 * w) * 4096 + dsel * 2048, w2b = w2a ^ 2048;
#define W2L(n) ld128_a<((n) & 1) * 1024>(W2[g2k((n) >> 2)][(n) & 3], vl, r0, (((n) & 2) ? w2a : w2b) + g2k((n) >> 2) * 4096)
  const int wso = OFF_W1F + cb * 114688 + w * 4096;
  const unsigned wm0 = lds0 + LDS_W1 + w * 4096;
#define W1P(q) do { if constexpr (((q) & 3) == 0) { set_m0<((q) >> 2) * 16384>(wm0); asm volatile("s_nop 0"); } dma16w<((q) & 3) * 1024>(vl, r0, wso + ((q) >> 2) * 16384); } while (0)
#define WST(n) do { if constexpr ((n) < 20) W1P(n); else W2L((n) - 20); } while (0)
#define POIS0(q) do { SB(); st128(PZ, r0, hb + ((q) >> 1) * 4096 + ((q) & 1) * 1024); SB(); } while (0)
  v4u uv0[2];
  v2u flate;
  auto poll = [&](v2u fl) {
#pragma unroll 1
    for (unsigned sp = 0; sp < SPIN_CAP && !__all(fl.x == tg.x && fl.y == tg.y); ++sp) {
      asm volatile("" ::: "memory");
      fl = __builtin_amdgcn_raw_buffer_load_b64(r0, t * FLS, OFF_FLAG + xcd * (256 * FLS), AUX_SC1);
    }
    __builtin_amdgcn_s_waitcnt(0x0F70);
    __builtin_amdgcn_fence(__ATOMIC_ACQUIRE, "wavefront");
    LDS_BAR();
    ld128x2_v(uv0[0], uv0[1], xio, r0, LB_UIN);
    if (b == 0 && t == 0) st64(tg, r0, OFF_EPOCH);
    SB();
  };
  {
    const int ch = t >> 7, n = t & 127;
    const float dt = rbf(expf(bf1(ldt_)));
    const float ar = -rbf(expf(bf1(lar_))) * dt, ai = bf1(aim_) * dt;
    const float cr = bf1(cr_), ci = bf1(ci_);
    unsigned mu = umax3(max(cr_ & 0x7FFFu, ci_ & 0x7FFFu), cr2_ & 0x7FFFu, ci2_ & 0x7FFFu) << 16;
    mu = max(mu, dppu<0x121>(mu)); mu = max(mu, dppu<0x122>(mu)); mu = max(mu, dppu<0x124>(mu)); mu = max(mu, dppu<0x128>(mu));
    unsigned pb_ = __builtin_bit_cast(unsigned, hmax(__builtin_bit_cast(float, mu)) * dt) & 0x7F800000u;
    if (pb_ == 0u || pb_ >= 0x7E800000u) pb_ = 0x3F800000u;
    const float inv = __builtin_bit_cast(float, 0x7F000000u - pb_);
    if (n == 0) *(LAS f32x2*)(lds + KG_RED + 512 + ch * 8) = f32x2{2.0f * __builtin_bit_cast(float, pb_), bf1(dsk_)};
    const f32x2 z1 = cexp_m(ar, -ai, 1.f), z8 = cexp_m(ar, ai, 8.f), z128 = cexp_m(ar, -ai, 128.f);
    LAS unsigned* Gs = (LAS unsigned*)LP(47);
    LAS unsigned* Es = (LAS unsigned*)LP(44);
    if (t == 0) *(LAS unsigned*)(lds + KG_KP + 8 * 4) = 0u;
    f32x2 Q[8];
    Q[1] = z1;
#pragma unroll
    for (int o = 2; o < 8; ++o) Q[o] = cmul(Q[o - 1], z1);
    f32x2 e = {1.f, 0.f}, P = {cr * dt * inv, -ci * dt * inv};
    static_for<0, 17>([&](auto HH) {
      constexpr int H = HH.v;
      if constexpr (H == WT_H) { PINV(P); SB(); wpub(); SB(); }
      if constexpr (H == FLAG_H) {
        PINV(P);
        SB();
        __builtin_amdgcn_s_waitcnt(0x0F70);
        WAITV0();
        PINV(codepf); PINV(tabpf); PINV(nearpf); PINV(b1pf);
        __builtin_amdgcn_s_barrier();
        if constexpr (FLS == 8) { if (t < 8) st64(tg, r0, OFF_FLAG + t * 2048 + b * 8); }
        else if (t < FLS / 2) st128(v4u{tg.x, tg.y, tg.x, tg.y}, r0, OFF_FLAG + (t / (FLS / 16)) * (256 * FLS) + b * FLS + (t % (FLS / 16)) * 16);
        SB();
      }
      if constexpr (H < 3) {
        SB();
        tget(IC<2 + 3 * H>{}); tget(IC<3 + 3 * H>{}); tget(IC<4 + 3 * H>{});
        SB();
      }
      constexpr int step = H < 2 ? 1 : H < 4 ? 2 : H < 8 ? 4 : 8;
      constexpr int row0 = H < 2 ? 8 * H : H < 4 ? 16 + 4 * (H - 2) : H < 8 ? 24 + 2 * (H - 4) : 32 + (H - 8);
#pragma unroll
      for (int o = 0; o < 8; o += step) {
        const f32x2 g = (o == 0) ? P : cmul(P, Q[o]);
        Gs[(row0 + o / step) * GS_ROW] = pk_f16(g.x, g.y);
      }
      if constexpr (H < 16) { P = cmul(P, z128); Es[H * GS_ROW] = pk_f16(e.x, e.y); e = (H == 0) ? z8 : cmul(e, z8); }
      if constexpr (H >= FLAG_H + 1 && (H - FLAG_H - 1) % 2 == 0 && (H - FLAG_H - 1) / 2 < NPK) POIS0((H - FLAG_H - 1) / 2);
    });
    static_assert(FLAG_H >= 3, "the table is complete behind the flag's wait");
    LDS_BAR();
    ksd = *(const LAS f32x2*)(lds + KG_RED + 512 + ((t >> 2) & 1) * 8);
    v2u fl;
    {
      const LAS v4u *ga = (const LAS v4u*)LP(45), *gb = (const LAS v4u*)LP(46), *eb = (const LAS v4u*)LP(43);
      v4u fa[8], fb[8], fe[8];
      f32x4 kk[2];
      static_for<0, 11>([&](auto N) {
        constexpr int z = N.v;
        if constexpr (z < 8) { fe[z] = eb[z * 4]; fa[z] = ga[z * 4]; fb[z] = gb[z * 4]; }
        if constexpr (z == 3) { MFMA_H0(kk[0], fa[0], fe[0]); MFMA_H0(kk[1], fb[0], fe[0]); }
        if constexpr (z > 3) { MFMA_H(kk[0], fa[z - 3], fe[z - 3]); MFMA_H(kk[1], fb[z - 3], fe[z - 3]); }
        if constexpr (z == POLL_Z && !POLL_LATE) fl = __builtin_amdgcn_raw_buffer_load_b64(r0, t * FLS, OFF_FLAG + xcd * (256 * FLS), AUX_SC1);
        SB();
      });
      asm volatile("s_nop 7" ::: "memory");
      PINV(kk[0]); PINV(kk[1]);
#pragma unroll
      for (int z = 0; z < 8; ++z) *(LAS _Float16*)LP(24 + z) = (_Float16)kk[z >> 2][z & 3];
    }
    SB();
    if constexpr (!POLL_LATE) poll(fl); else { LDS_BAR(); flate = __builtin_amdgcn_raw_buffer_load_b64(r0, t * FLS, OFF_FLAG + xcd * (256 * FLS), AUX_SC1); }
#pragma unroll
    for (int i = 0; i < 11; ++i) kv[i] = *(const LAS unsigned*)LP(32 + i);
    SB();
    if constexpr (!POLL_LATE) { WST(0); st128(PZ, r0, LB_BYTES + LB_UIN + xro); SB(); }
  }
  LAS u8 *pk = LP(8), *pz = LP(9), *pex = LP(13), *pu = LP(15), *pin1 = LP(20), *pin2 = LP(21), *pin3 = LP(22);
  const LAS u8 *pa = LP(10), *pa2 = LP(11), *pb = LP(12), *px0 = LP(16), *px1 = LP(17), *pred = LP(23);
  const int vo0 = TI(18), vo1 = TI(19);
  auto expand_k = [&](auto FIRST, auto&& mid) {
    if constexpr (!FIRST.v) {
#pragma unroll
      for (int i = 0; i < 11; ++i) PINV(kv[i]);
    }
    static_for<0, 2>([&](auto E) {
      constexpr int e = E.v;
      if constexpr (e == 1) { SB(); mid(); SB(); }
      unsigned pr[10];
#pragma unroll
      for (int i = 0; i < 10; ++i) pr[i] = __builtin_amdgcn_perm(kv[i + 1], kv[i], e ? 0x07060302u : 0x05040100u);
      static_for<0, 4>([&](auto CP) {
        constexpr int cp = CP.v;
        *(LAS v4u*)(pk + e * KC_CH + cp * KC_STRIDE) = v4u{pr[cp], pr[cp + 2], pr[cp + 4], pr[cp + 6]};
        if constexpr (FIRST.v && !POLL_LATE) WST(1 + 4 * e + cp);
      });
    });
    *(LAS v4u*)pz = v4u{0u, 0u, 0u, 0u};
  };
  const LAS u8 *pe0 = LP(14) + 256 * sw, *pe1 = LP(14) + 256 - 256 * sw;
  expand_k(IC<1>{}, [] {});
  SB();
  if constexpr (POLL_LATE) { poll(flate); static_for<0, 9>([&](auto N) { WST(N.v); }); st128(PZ, r0, LB_BYTES + LB_UIN + xro); }
  st128(PZ, r0, LB_BYTES + LB_UIN + xro + 1024);
  SB();
  static_for<0, 2>([&](auto LL) {
    constexpr int layer = LL.v;
    const __amdgpu_buffer_rsrc_t rs = __builtin_amdgcn_make_buffer_rsrc((void*)(p.ws + (size_t)layer * LB_BYTES), 0, WS_BYTES - LB_BYTES, RSRC_W3);
    int xs0 = LB_X1 + g1 * 32768, pso = LB_PS1 + g1 * 4096, hs0 = LB_H + (tb * 56 + 14 * w) * 4096;
    const __amdgpu_buffer_rsrc_t rh = (HSHARE || layer == 0) ? r0 : __builtin_amdgcn_make_buffer_rsrc((void*)(p.ws + H_BYTES), 0, WS_BYTES - H_BYTES, RSRC_W3);
#define POIS(q) do { if ((q) < 6) st128(PZ, rh, hb + ((q) >> 1) * 4096 + ((q) & 1) * 1024); else st128(PZ, rh, hodd); } while (0)
      f32x4 acc[8];
      v2u Al[2][8], Ah[2][8];
      v4u B[2][8];
#define CV_RD(buf, si, r) do { const int g_ = (r) / 3, oa_ = 64 * ((3 - (si)) + 8 * (7 - g_)); \
        if ((r) % 3 == 0) B[buf][g_] = *(const LAS v4u*)(pb + 64 * (3 - (si)) + 512 * g_); \
        else if ((r) % 3 == 1) Al[buf][g_] = *(const LAS v2u*)(pa + oa_); \
        else Ah[buf][g_] = *(const LAS v2u*)(pa2 + oa_); } while (0)
#define CV_A(buf, g) v4u{Al[buf][g].x, Al[buf][g].y, Ah[buf][g].x, Ah[buf][g].y}
    v4u uv[2], xv[2], bq[4];
    const __amdgpu_buffer_rsrc_t rb1 = __builtin_amdgcn_make_buffer_rsrc((void*)p.b1, 0, F_ * 2, RSRC_W3);
    if constexpr (layer == 0) {
      waitv<WS_P0 + 2>();
      PINV(uv0[0]); PINV(uv0[1]);
      uv[0] = uv0[0]; uv[1] = uv0[1];
    } else {
      uv[0] = __builtin_amdgcn_raw_buffer_load_b128(rs, xio, LB_UIN, U_AUX);
      uv[1] = __builtin_amdgcn_raw_buffer_load_b128(rs, xio, LB_UIN + XHI, U_AUX);
    }
    SB();
    if constexpr (APRE && layer > 0) {
      LDS_BAR();
#pragma unroll
      for (int r = 0; r < 12; ++r) if (r % 3 != 0) { CV_RD(0, 0, r); SB(); }
    }
    if constexpr (layer > 0) {
#pragma unroll 1
      for (unsigned sp = 0; sp < SPIN_CAP && __any(umax4(umax4(0u, uv[0]), uv[1]) == POISON); ++sp) {
        if (U_RS) __builtin_amdgcn_s_sleep(U_RS);
        asm volatile("" ::: "memory");
        uv[0] = __builtin_amdgcn_raw_buffer_load_b128(rs, xio, LB_UIN, U_AUX);
        uv[1] = __builtin_amdgcn_raw_buffer_load_b128(rs, xio, LB_UIN + XHI, U_AUX);
      }
    }
    *(LAS v4u*)pu = uv[0];
    *(LAS v4u*)(pu + 2048) = uv[1];
    ld128x2_v(xv[0], xv[1], xio, rs, LB_XIN);
    LDS_BAR();
    {
#define CV_W1(m) do { if constexpr (layer == 0 && (m) % CV_D1 == 2 && W1_P0 + (m) / CV_D1 < 28) W1P(W1_P0 + (m) / CV_D1); if constexpr (layer == 0 && cv_w2_before((m) + 1) > cv_w2_before(m)) W2L(W2_PRE + cv_w2_before(m)); } while (0)
#define CV_PO(m) do { if constexpr (layer == 0 && (m) >= P0_0 && ((m) - P0_0) % P0_D == 0 && ((m) - P0_0) / P0_D < 7 - NPK) POIS(NPK + ((m) - P0_0) / P0_D); if constexpr (layer == 1 && (m) >= PO_0 && ((m) - PO_0) % PO_D == 0 && ((m) - PO_0) / PO_D < 7) POIS(((m) - PO_0) / PO_D); } while (0)
#pragma unroll
      for (int r = 0; r < 12; ++r) if (!(APRE && layer > 0) || r % 3 == 0) { CV_RD(0, 0, r); SB(); }
      if (STAG & 1) stagger(w);
      static_for<0, 108>([&](auto M) {
        constexpr int m = M.v, cur = (m / 36) & 1, ga = CVS.ga[m], gb = CVS.gb[m], rd = CVS.rd[m];
        if constexpr (CVS.first[m]) MFMA_H0(acc[ga + gb], CV_A(cur, ga), B[cur][gb]); else MFMA_H(acc[ga + gb], CV_A(cur, ga), B[cur][gb]);
        SB();
        if constexpr (rd >= 0) CV_RD((rd / 24) & 1, rd / 24, rd % 24);
        CV_W1(m);
        CV_PO(m);
        if constexpr (m < 16 && (m & 3) == 0) {
          constexpr int i = m >> 2;
          ld128_p(bq[i], h16, rb1, (7 * (cb >> 1) + (i < 3 ? 3 * (cb & 1) + i : 6)) * 64);
        }
        SB();
      });
      waitv<layer == 0 ? cv_po_between(13, 108) + cv_w2_before(108) + cv_w1_between(13, 108) : (107 - PO_0) / PO_D + 1 < 7 ? (107 - PO_0) / PO_D + 1 : 7>();
      LDS_BAR();
      PINV(xv[0]); PINV(xv[1]); PINV(bq[0]); PINV(bq[1]); PINV(bq[2]); PINV(bq[3]);
      if (sw) {
#pragma unroll
        for (int e = 0; e < 2; ++e) { uv[e] = uv[e].zwxy; xv[e] = xv[e].zwxy; }
      }
      if (STAG & 4) stagger(w);
      SB();
      float base[2][8], bu;
      static_assert(2 * cvl_free(36) >= 48, "all elements");
      static_for<0, 36>([&](auto M) {
        constexpr int m = M.v, tl = cvl_tile(m), g = m - tl * (tl + 1) / 2;
        MFMA_H(acc[tl], CV_A(1, g), B[1][tl - g]);
        SB();
        if constexpr (m >= 9 && m <= 29 && (m - 9) % 4 == 0) *(LAS f32x4*)(pex + ((m - 9) / 4) * EX_TILE) = acc[(m - 9) / 4];
        else if constexpr (m == 35) *(LAS f32x4*)(pex + 6 * EX_TILE) = acc[6];
        else {
          static_for<2 * cvl_free(m), 2 * cvl_free(m) + 2>([&](auto Z) {
            constexpr int el = Z.v / 3, st = Z.v % 3, e = el >> 3, k = el & 7;
            if constexpr (el < 16) {
              const unsigned xx = xv[e][k >> 1], uu = uv[e][k >> 1];
              if constexpr (st == 0) bu = (k & 1) ? i_cvt_hi(uu) : i_cvt_lo(uu);
              else if constexpr (st == 1) { base[e][k] = (k & 1) ? bf_hi(xx) : bf_lo(xx); PINV(base[e][k]); }
              else { base[e][k] = fmaf(bu, ksd.y, base[e][k]); PINV(base[e][k]); }
            }
          });
        }
        CV_W1(108 + m);
        CV_PO(108 + m);
        SB();
      });
      asm volatile("s_nop 6" ::: "memory");
      SB();
      *(LAS f32x4*)(pex + 7 * EX_TILE) = acc[7];
      if constexpr (layer == 0) waitv<W2_CV + cv_po_between(CV_M2 - 4, 144)>();
      LDS_BAR();
      f32x4 ya[2][2], yb[2][2];
#pragma unroll
      for (int e = 0; e < 2; ++e)
#pragma unroll
        for (int q = 0; q < 2; ++q) { ya[e][q] = *(const LAS f32x4*)(pe0 + (4 * e + 8 * q) * EX_TILE); yb[e][q] = *(const LAS f32x4*)(pe1 + (4 * e + 8 * q) * EX_TILE); }
      SB();
#pragma unroll
      for (int e = 0; e < 2; ++e) {
        const float sc = ksd.x;
        const f32x4 y0 = ya[e][0] + ya[e][1], y1 = yb[e][0] + yb[e][1];
        const v4u o = {pk_bf16(base[e][0] + y0[0] * sc, base[e][1] + y0[1] * sc), pk_bf16(base[e][2] + y0[2] * sc, base[e][3] + y0[3] * sc),
                       pk_bf16(base[e][4] + y1[0] * sc, base[e][5] + y1[1] * sc), pk_bf16(base[e][6] + y1[2] * sc, base[e][7] + y1[3] * sc)};
        st128(san4(o), rs, xio + e * XHI);
      }
    }
    v4u A[G2_WIN], xq[2];
#define H_LOAD(n) ld128_v<g2_imm(n)>(A[(n) % G2_WIN], (g2_lrow(n) & 1) ? vo1 : vo0, rh, hs0 + g2_ks(n) * 4096)
    {
      unsigned ring = lds0 + LDS_SCR + w * RING_W;
#define X_M0(sl) set_m0<((sl) % RD) * 2048>(ring)
#define X_DA(so) dma16<0>(vl, rs, so)
#define X_DB(so) dma16<1024>(vl, rs, so)
      v4u WA[7];
      v2u Xl[3][2], Xh[3][2];
#define W_READ(ks, i) WA[i] = *(const LAS v4u*)(((ks) < 8 ? pw0 : pw1) + (((ks) & 7) * 7 + (i)) * 1024)
#define X_RD(buf, sl, r) do { const LAS u8* q_ = (((r) >> 1) ? px1 : px0) + ((sl) % RD) * 2048; \
        if (((r) & 1) == 0) Xl[buf][(r) >> 1] = tr_read(q_); else Xh[buf][(r) >> 1] = tr_read(q_ + 256); } while (0)
#define X_B(buf, i) v4u{Xl[buf][i].x, Xl[buf][i].y, Xh[buf][i].x, Xh[buf][i].y}
      f32x4 acc[7][2];
      v4u ps[4], ones = {0x3F803F80u, 0x3F803F80u, 0x3F803F80u, 0x3F803F80u};
      PINV(ones);
      SB();
      f32x4 bbv[7];
#pragma unroll
      for (int i = 0; i < 7; ++i) {
#pragma unroll
        for (int z = 0; z < 4; ++z) {
          const unsigned u = bq[i >> 1][z];
          bbv[i][z] = (i == 6 ? bf_hi(u << (16 - 16 * (cb & 1))) : (i & 1) ? bf_hi(u) : bf_lo(u)) * 1.44269504088896f;
        }
        PINV(bbv[i]);
      }
      SB();
      if constexpr (layer == 0) {
        const int fo0 = w * 1024, fo1 = (w < 3 ? w + 4 : 6) * 1024;
        const LAS u8* qa0 = pw0 + fo0; const LAS u8* qa1 = pw1 + fo0; const LAS u8* qb0 = pw0 + fo1; const LAS u8* qb1 = pw1 + fo1;
        f32x4 c0, c1;
        v4u fr[8];
        static_for<0, 40>([&](auto N) {
          constexpr int n = N.v;
          if constexpr (n >= 8) {
            if constexpr (n < 10) MFMA_V0((n & 1) ? c1 : c0, fr[n & 7], ones); else MFMA_V((n & 1) ? c1 : c0, fr[n & 7], ones);
            SB();
          }
          if constexpr (n < 32) fr[n & 7] = *(const LAS v4u*)(((n & 1) ? (n < 16 ? qb0 : qb1) : (n < 16 ? qa0 : qa1)) + ((n >> 1) & 7) * 7168);
          if constexpr (n % (32 / W2_CS) == 1 && n / (32 / W2_CS) < W2_CS) W2L(W2_P0 + n / (32 / W2_CS));
          SB();
        });
        asm volatile("s_nop 7" ::: "memory");
        SB();
        if (c == 0) { *(LAS f32x4*)(lds + CS_OFF + (fo0 >> 4) + h * 16) = c0; *(LAS f32x4*)(lds + CS_OFF + (fo1 >> 4) + h * 16) = c1; }
        LDS_BAR();
#pragma unroll
        for (int i = 0; i < 7; ++i) csa[i] = *(const LAS f32x4*)(lds + CS_OFF + i * 64 + h * 16);
      }
      waitv<layer == 0 ? W2_CS : 0>();
      LDS_BAR();
      SB();
      if (layer == 1 && G1_DELAY) __builtin_amdgcn_s_sleep(G1_DELAY);
      if (layer == 0 && G0_DELAY) __builtin_amdgcn_s_sleep(G0_DELAY);
      unsigned tries = 0;
      unsigned long long live = ~0ull;
      int pst = h == (c >> 2) ? pso + cb * 256 + c * 16 : DROP;
      unsigned long long mk1 = 0xAAAAAAAAAAAAAAAAull, mk2 = 0xCCCCCCCCCCCCCCCCull;
      PINS(mk1); PINS(mk2);
      float fa[4], fb[4], rr[2], rm[2];
      PINV(pst);
      unsigned thr = 0xFFFEFFFFu;
      int xsS = xs0 + cb * 2048;
#define X_CHECK(id, buf, sl, k0, k1, skip) do { slow_begin<id>(); \
        static_for<(k0), (k1) + 1>([&](auto K) { if constexpr (K.v == (skip)) slow_dma<0, (K.v % RD) * 2048>(vl, rs, xsS, ring); else slow_dma<K.v * 2048, (K.v % RD) * 2048>(vl, rs, xs0, ring); }); \
        check_x<id, ((sl) % RD) * 2048>(lm, tries, thr, Xl[buf][0], Xh[buf][0], Xl[buf][1], Xh[buf][1], px0, px1); \
        PINV(Xl[buf][0]); PINV(Xh[buf][0]); PINV(Xl[buf][1]); PINV(Xh[buf][1]); SB(); } while (0)
#define X_MAX(buf) umax3(max(Xl[buf][0].x, Xl[buf][0].y), Xh[buf][0].x, Xh[buf][0].y)
#define X_RD4(buf, sl) do { X_RD(buf, sl, 0); X_RD(buf, sl, 1); X_RD(buf, sl, 2); X_RD(buf, sl, 3); } while (0)
      for (;;) {
        PINS(xs0); PINS(xsS); PINS(ring);
        unsigned vm;
        unsigned long long lm;
        static_for<0, 6>([&](auto K) {
          constexpr int sl = K.v < 2 ? K.v : K.v == 2 ? 5 : K.v - 1;
          const int so = sl < 5 ? xs0 + sl * 2048 : xsS;
          X_M0(sl); asm volatile("s_nop 0"); X_DA(so); X_DB(so);
        });
#pragma unroll
        for (int i = 0; i < 7; ++i) W_READ(0, i);
        waitv<2 * (RD - 1)>();
        X_RD4(0, 0);
        lm = late_x(X_MAX(0), thr);
        SB();
        X_CHECK(layer * 100 + 20, 0, 0, 0, 5, 5);
        SB();
        unsigned long long badm;
        f32x4 sg[4];
        v4u pv;
        int pa_;
        auto pub = [&](auto N) {
          constexpr int n = N.v;
          if constexpr (n == 0) { PINV(sg[0]); PINV(sg[2]); }
          if constexpr (n < 4) fa[n] = i_sel(sg[1 + (n & 2)][2 * (n & 1)], sg[1 + (n & 2)][2 * (n & 1) + 1], mk1);
          else if constexpr (n < 6) fb[n - 4] = i_sel(fa[2 * (n - 4)], fa[2 * (n - 4) + 1], mk2);
          else if constexpr (n == 6) { const float f_ = sg[0].x; unsigned u = san(__builtin_bit_cast(unsigned, f_)); PINV(u); pv.x = u; }
          else if constexpr (n == 7) { const float f_ = sg[2].x; unsigned u = san(__builtin_bit_cast(unsigned, f_)); PINV(u); pv.z = u; }
          else if constexpr (n == 8) { badm = __builtin_amdgcn_ballot_w64(__builtin_isunordered(sg[0].x, sg[2].x)) & live; PINS(badm); }
          else if constexpr (n == 9) { unsigned u = san(__builtin_bit_cast(unsigned, fb[0])); PINV(u); pv.y = u; }
          else if constexpr (n == 10) { unsigned u = san(__builtin_bit_cast(unsigned, fb[1])); PINV(u); pv.w = u; }
          else if constexpr (n == 11) { pa_ = badm == 0ull ? pst : DROP; PINV(pa_); }
        };
        auto fin = [&](auto N) {
          constexpr int n = N.v, z = n & 3;
          auto P = [&](int i) { const unsigned u = ps[i][z]; return __builtin_bit_cast(float, u); };
          if constexpr (n < 4) fa[z] = i_add(P(0), P(1));
          else if constexpr (n < 8) fb[z] = i_add(P(2), P(3));
          else if constexpr (n < 12) fa[z] = i_add(fa[z], fb[z]);
          else if constexpr (n < 16) fb[z] = i_mov(fa[z]);
          else if constexpr (n < 20) i_sw16(fa[z], fb[z]);
          else if constexpr (n < 24) fa[z] = i_add(fa[z], fb[z]);
          else if constexpr (n < 28) fb[z] = i_mov(fa[z]);
          else if constexpr (n < 32) i_sw32(fa[z], fb[z]);
          else if constexpr (n < 36) fa[z] = i_add(fa[z], fb[z]);
          else if constexpr (n < 52) {
            constexpr int e = n & 1, st = (n - 36) >> 1;
            if constexpr (st == 0) fa[2 * e] = fa[2 * e] * (1.f / D_);
            else if constexpr (st == 1) fb[e] = fa[2 * e] * fa[2 * e];
            else if constexpr (st == 2) fb[e] = fmaf(fa[2 * e + 1], 1.f / D_, -fb[e]);
            else if constexpr (st == 3) fb[e] = __builtin_amdgcn_fmed3f(fb[e], 0.f, 3.0e38f);
            else if constexpr (st == 4) fb[e] = fb[e] + 1e-5f;
            else if constexpr (st == 5) fb[e] = __builtin_amdgcn_rsqf(fb[e]);
            else if constexpr (st == 6) rr[e] = fb[e] * 1.44269504088896f;
            else rm[e] = -rr[e] * fa[2 * e];
            PINV(fa[2 * e]); PINV(fb[e]);
            if constexpr (st >= 6) PINV(rr[e]);
            if constexpr (st == 7) PINV(rm[e]);
          }
        };
        static_assert(2 * (g1_slot0(layer, 1, ST_K, 0) - g1_slot0(layer, 1, 1, 4)) >= 12 && PS_K + 3 >= 10 && FIN_K > PS_K + 3, "publish fits; the last partial-sum block is younger than every x piece");
        static_for<0, 16 * 14>([&](auto M) {
          constexpr int ks = M.v / 14, q = M.v % 14, cur = ks & 1, i = q >> 1, tt = q & 1;
          if constexpr (ks == 1 && q == 0) {
            X_CHECK(layer * 100 + 21, 2, 5, 2, 6, 5);
            MFMA_V0(sg[0], ones, X_B(2, 0)); X_M0(5);
            MFMA_V0(sg[1], X_B(2, 0), X_B(2, 0)); X_DA(xs0 + 5 * 2048);
            MFMA_V0(sg[2], ones, X_B(2, 1)); X_DB(xs0 + 5 * 2048);
            MFMA_V0(sg[3], X_B(2, 1), X_B(2, 1));
            SB();
          }
          if constexpr (ks == 0) MFMA_V0(acc[i][tt], WA[i], X_B(cur, tt)); else MFMA_V(acc[i][tt], WA[i], X_B(cur, tt));
          SB();
          if constexpr (layer == 0 && q < 8 && g1_w2n(ks, q) >= 0) W2L(g1_w2n(ks, q));
          if constexpr (q == 0) {
            if constexpr (ks >= PS_K && ks < PS_K + 4) ld128_v<(ks - PS_K) * 1024>(ps[ks - PS_K], vl, rs, pso);
            if constexpr (ks == ST_K) st128(pv, rs, pa_);
            if constexpr (ks == FIN_K) { waitv<g1_wait(layer, ks, 0, 17)>(); PINV(ps[0]); PINV(ps[1]); PINV(ps[2]); PINV(ps[3]); }
            else if constexpr (ks < 15 && ks < FIN_K) waitv<g1_wait(layer, ks, 0, ks + 1)>();
          }
          else if constexpr (q == 1) { if constexpr (ks < 15) W_READ(ks + 1, 0); }
          else if constexpr (q < 6) { if constexpr (ks < 15) X_RD(cur ^ 1, ks + 1, q - 2); if constexpr (q == (ks >= ST_K ? 4 : 3) && ks + RD < 16) X_M0(ks + RD); }
          else if constexpr (q == 6) { if constexpr (ks + RD < 16) X_DA(xs0 + (ks + RD) * 2048); }
          else if constexpr (q == 7) { if constexpr (ks + RD < 16) X_DB(xs0 + (ks + RD) * 2048); if constexpr (ks < 15) __builtin_amdgcn_s_waitcnt(0xC27F); if constexpr (ks == 0) waitv<g1_wait(layer, 0, 7, 16)>(); }
          else if constexpr (ks < 15) {
            if constexpr (ks == 0 && q < 12) X_RD(2, 5, q - 8);
            W_READ(ks + 1, q - 7);
            if constexpr (q == 8 && ks < CHK_K) { vm = max(Xl[cur ^ 1][0].x, Xl[cur ^ 1][0].y); PINV(vm); }
            if constexpr (q == 9 && ks < CHK_K) { vm = umax3(vm, Xh[cur ^ 1][0].x, Xh[cur ^ 1][0].y); PINV(vm); }
            if constexpr (q == 10 && ks < CHK_K) lm = late_x(vm, thr);
            if constexpr (q == 12 && ks >= ST_K && ks < CHK_K) __builtin_amdgcn_s_waitcnt(0xC57F);
            if constexpr (q == 13 && ks < CHK_K) {
              SB();
              X_CHECK(layer * 100 + ks, cur ^ 1, ks + 1, ks + 1, (ks + RD < 16 ? ks + RD : 15), (ks == 0 ? 5 : -1));
              if constexpr (ks == 0) lm = late_x(X_MAX(2), thr);
            }
          }
          if constexpr (layer == 0 && (PREX & 1) && (M.v == 13 * 14 + 3 || M.v == 14 * 14 + 3)) st128(PZ, r0, LB_BYTES + LB_XIN + xro + (ks - 13) * 1024);
          if constexpr (layer == 0 && (PREX & 2) && (M.v == 13 * 14 + 13 || M.v == 14 * 14 + 13)) st128(PZ, r0, LB_BYTES + xio + (ks - 13) * XHI);
          if constexpr (M.v >= 14 + 4 && ks < ST_K) static_for<2 * (g1_slot0(layer, 1, ks, q) - g1_slot0(layer, 1, 1, 4)), 2 * (g1_slot0(layer, 1, ks, q + 1) - g1_slot0(layer, 1, 1, 4))>(pub);
          if constexpr (ks >= FIN_K) static_for<g1_slot0(layer, FIN_K, ks, q), g1_slot0(layer, FIN_K, ks, q + 1)>(fin);
          SB();
        });
        static_for<g1_slot0(layer, FIN_K, 16, 0), 52>(fin);
        if (__builtin_expect(((__builtin_amdgcn_ballot_w64(__builtin_isunordered(acc[0][0].x, acc[0][1].x)) & live) | badm) == 0ull, 1)) break;
        WAITV0();
        for (unsigned sp = 0; sp < SPIN_CAP; ++sp) {
          unsigned vv = 0u;
          asm volatile("" ::: "memory");
#pragma unroll 4
          for (int i = 0; i < 32; ++i) vv = umax4(vv, ld128(rs, vl, xs0 + i * 1024));
          if (!__any(vv == POISON)) break;
        }
        live = 0ull; thr = ~0u;
        __builtin_amdgcn_s_waitcnt(0x0070);
      }
      if (__builtin_expect(__any(__builtin_isunordered(rm[0] + fa[1], rm[1] + fa[3])), 0)) {
#pragma unroll 1
        for (unsigned sp = 0; sp < SPIN_CAP && __any(umax4(umax4(umax4(umax4(0u, ps[0]), ps[1]), ps[2]), ps[3]) == POISON); ++sp) {
          ld128_v<0>(ps[0], vl, rs, pso); ld128_v<1024>(ps[1], vl, rs, pso); ld128_v<2048>(ps[2], vl, rs, pso); ld128_v<3072>(ps[3], vl, rs, pso);
          WAITV0();
          PINV(ps[0]); PINV(ps[1]); PINV(ps[2]); PINV(ps[3]);
        }
        const f32x4 pv = (__builtin_bit_cast(f32x4, ps[0]) + __builtin_bit_cast(f32x4, ps[1])) + (__builtin_bit_cast(f32x4, ps[2]) + __builtin_bit_cast(f32x4, ps[3]));
        const float s0 = hsum(pv.x), q0 = hsum(pv.y), s1 = hsum(pv.z), q1 = hsum(pv.w);
        const float m0 = s0 * (1.f / D_), m1 = s1 * (1.f / D_);
        rr[0] = __builtin_amdgcn_rsqf(__builtin_amdgcn_fmed3f(fmaf(q0, 1.f / D_, -(m0 * m0)), 0.f, 3.0e38f) + 1e-5f) * 1.44269504088896f;
        rr[1] = __builtin_amdgcn_rsqf(__builtin_amdgcn_fmed3f(fmaf(q1, 1.f / D_, -(m1 * m1)), 0.f, 3.0e38f) + 1e-5f) * 1.44269504088896f;
        rm[0] = -rr[0] * m0; rm[1] = -rr[1] * m1;
      }
      __builtin_amdgcn_s_barrier();
      asm volatile("" ::: "memory");
      const f32x2 r2[2] = {f32x2{rr[0], rr[0]}, f32x2{rr[1], rr[1]}}, rm2[2] = {f32x2{rm[0], rm[0]}, f32x2{rm[1], rm[1]}};
      if (STAG & 2) stagger(w);
      ld128_v<0>(xq[0], xro, rs, LB_X1); ld128_v<1024>(xq[1], xro, rs, LB_X1);
      SB();
      constexpr int MW = W2_AFTER_LOOP + 3 * EARLY_M >= 56 ? 3 : 4;
      static_for<0, 7>([&](auto M) {
        constexpr int tt = M.v >> 2, pr = M.v & 3, np = 4;
#define M_TI(z) (pr < 3 ? 2 * pr + ((z) >> 1) : 6)
#define M_TT(z) (pr < 3 ? tt : (z) >> 1)
        const f32x2 two = {2.f, 2.f}, c1 = {-1.38629436111989f, -1.38629436111989f}, c2 = {0.693147180559945f, 0.693147180559945f};
        f32x2 hl[4], e[4];
        unsigned o[4];
#pragma unroll
        for (int z = 0; z < np; ++z) {
          const f32x4 cs = csa[M_TI(z)], bb = bbv[M_TI(z)];
          e[z] = (z & 1) ? pkfma(rm2[M_TT(z)], cs.zw, bb.zw) : pkfma(rm2[M_TT(z)], cs.xy, bb.xy);
        }
        SB();
#pragma unroll
        for (int z = 0; z < np; ++z) hl[z] = pkfma(r2[M_TT(z)], (z & 1) ? acc[M_TI(z)][M_TT(z)].zw : acc[M_TI(z)][M_TT(z)].xy, e[z]);
        SB();
        if constexpr (layer == 0 && W2_AFTER_LOOP + MW * M.v < 56) W2L(W2_AFTER_LOOP + MW * M.v);
#pragma unroll
        for (int z = 0; z < np; ++z) e[z] = f32x2{__builtin_amdgcn_exp2f(hl[z].x), __builtin_amdgcn_exp2f(hl[z].y)};
        SB();
        f32x2 a[4];
#pragma unroll
        for (int z = 0; z < np; ++z) a[z] = e[z] + two;
        SB();
#pragma unroll
        for (int z = 0; z < np; ++z) e[z] = pkfma(e[z], a[z], two);
        SB();
        if constexpr (layer == 0 && W2_AFTER_LOOP + MW * M.v + 1 < 56) W2L(W2_AFTER_LOOP + MW * M.v + 1);
        if constexpr (M.v >= EARLY_M) { H_LOAD(3 * (M.v - EARLY_M)); }
#pragma unroll
        for (int z = 0; z < np; ++z) e[z] = f32x2{__builtin_amdgcn_rcpf(e[z].x), __builtin_amdgcn_rcpf(e[z].y)};
        SB();
        if constexpr (M.v >= EARLY_M) { H_LOAD(3 * (M.v - EARLY_M) + 1); }
        if constexpr (layer == 0 && W2_AFTER_LOOP + MW * M.v + 2 < 56) W2L(W2_AFTER_LOOP + MW * M.v + 2);
#pragma unroll
        for (int z = 0; z < np; ++z) e[z] = pkfma(e[z], c1, c2);
        SB();
#pragma unroll
        for (int z = 0; z < np; ++z) e[z] = e[z] * hl[z];
        SB();
        if constexpr (layer == 0 && MW == 4 && W2_AFTER_LOOP + 4 * M.v + 3 < 56) W2L(W2_AFTER_LOOP + 4 * M.v + 3);
        if constexpr (M.v >= EARLY_M) { H_LOAD(3 * (M.v - EARLY_M) + 2); }
#pragma unroll
        for (int z = 0; z < np; ++z) o[z] = pk_bf16(e[z].x, e[z].y);
        SB();
#pragma unroll
        for (int z = 0; z < np; ++z) o[z] = san(o[z]);
        st128(v4u{o[0], o[1], o[2], o[3]}, rh, pr < 3 ? hb + pr * 4096 + tt * 1024 : hodd);
        SB();
      });
      static_assert(cv_w2_before(144) == W2_CV && W2_AFTER_LOOP + MW * EARLY_M >= 56 && cv_w1_between(0, 144) == 28 - W1_P0, "all w2 fragments are requested");
      SB();
      static_for<3 * (7 - EARLY_M), G2_WIN>([&](auto N) { H_LOAD(N.v); });
      SB();
    }
    {
      f32x4 acc[4][4], rr[6];
      unsigned pr[8];
      const __amdgpu_buffer_rsrc_t ro = __builtin_amdgcn_make_buffer_rsrc((void*)p.out, 0, XP_BYTES, RSRC_W3);
      int oo = h32 * 256 + (vl & 0xF0) / 4;
      if constexpr (layer == 1) PINV(oo);
#define OUT_ST(k) __builtin_amdgcn_raw_buffer_store_b32(pr[k], ro, oo + ((k) & 3) * 1024, g2 * 32768 + (32 * db + 16 * dsel) * 4 + ((k) >> 2) * 4096, 0)
      unsigned vm = 0u, tries = 0;
      constexpr int NE = 3 * (7 - EARLY_M);
#define H_WAIT(f, issued) waitv<((issued) < 56 ? (issued) : 56) + (layer == 0 && !(PREX & 2) ? ((issued) > 56) + ((issued) > 59) : 0) - 1 - (f) + ((f) < NE ? (7 - EARLY_M) - (f) / 3 : 0)>()
#define H_CHECK(f0) do { SB(); slow_begin<layer * 100 + 30 + (f0) / 4>(); \
        static_for<(f0), ((f0) + G2_WIN < 56 ? (f0) + G2_WIN : 56)>([&](auto N) { \
          slow_ld<g2_ks(N.v) * 4096, g2_imm(N.v)>(A[N.v % G2_WIN], (g2_lrow(N.v) & 1) ? vo1 : vo0, rh, hs0); }); \
        check_h<layer * 100 + 30 + (f0) / 4>(vm, tries, A[(f0) % G2_WIN], A[((f0) + 1) % G2_WIN], A[((f0) + 2) % G2_WIN], A[((f0) + 3) % G2_WIN]); \
        PINV(A[(f0) % G2_WIN]); PINV(A[((f0) + 1) % G2_WIN]); PINV(A[((f0) + 2) % G2_WIN]); PINV(A[((f0) + 3) % G2_WIN]); SB(); } while (0)
      static_assert(G2C == 4, "check_h takes four fragments");
      PINS(hs0);
      H_WAIT(G2C - 1, G2_WIN);
      SB();
      static_for<0, G2C>([&](auto N) { vm = umax4(vm, A[N.v]); });
      H_CHECK(0);
      SB();
      static_for<0, 224>([&](auto M) {
        constexpr int n = M.v >> 2, j = M.v & 3, s = g2_lrow(n), k = g2_idx(n);
        if constexpr (g2_first(n) && !((s & 1) && j >= 2)) MFMA_A0(acc[g2_arow(n)][j], A[n % G2_WIN], W2[g2_ks(n)][j]); else MFMA_A(acc[g2_arow(n)][j], A[n % G2_WIN], W2[g2_ks(n)][j]);
        SB();
        if constexpr (n + G2C < 56) {
          if constexpr (j == 0) H_WAIT(n + G2C, n + G2_WIN);
          if constexpr (j == 2) { vm = umax4(n % G2C == 0 ? 0u : vm, A[(n + G2C) % G2_WIN]); PINV(vm); }
        }
        if constexpr (j == 3) {
          if constexpr (n + G2_WIN < 56) H_LOAD(n + G2_WIN);
          else if constexpr (layer == 0 && !(PREX & 2) && (n + G2_WIN == 56 || n + G2_WIN == 59)) st128(PZ, r0, LB_BYTES + xio + (n + G2_WIN == 56 ? 0 : XHI));
          if constexpr ((n + 1) % G2C == 0 && n + 1 < 56) H_CHECK(n + 1);
          if constexpr (n == 11 || n == 27) { swap_halves(A[(n + 1) % G2_WIN], A[(n + 2) % G2_WIN]); swap_halves(A[(n + 3) % G2_WIN], A[(n + 4) % G2_WIN]); asm volatile("s_nop 1"); }
        }
        if constexpr (j == 1 && s > 0 && (k & 1) == 1 && k >= 3 && ((k - 3) >> 1) < (((s - 1) & 1) ? 2 : 4)) {
          constexpr int sp = s - 1, jj = (k - 3) >> 1;
          LAS u8* dst = (sp & 1) ? pin1 : (jj >= 2 ? pin2 : pin3);
          *(LAS f32x4*)(dst + ((sp >> 1) * 2 + (jj & 1)) * 1024) = acc[sp][jj];
        }
        if constexpr (j == 1 && n >= 1 && n < 9) {
          constexpr int z = n - 1, i = z >> 2, jj = (z >> 1) & 1, d = z & 1;
          if constexpr (n == 1) { PINV(xq[0]); PINV(xq[1]); }
          const unsigned u = xq[jj][(jj ? 2 - 2 * i : 2 * i) + d];
          float lo = bf_lo(u), hi = bf_hi(u);
          PINV(lo); PINV(hi);
          acc[2 * i + 1][2 + jj][2 * d] = lo; acc[2 * i + 1][2 + jj][2 * d + 1] = hi;
        }
        if constexpr (EH) {
          if constexpr (n == 40 && j == 1) LDS_BAR();
          if constexpr (n >= 41 && n < 44 && (j == 0 || j == 1)) { constexpr int z = (n - 41) * 2 + j; rr[z] = *(const LAS f32x4*)(pred + ((z >> 1) * 4 + (z & 1)) * 1024); }
          if constexpr (g2_eh(M.v + 1) > g2_eh(M.v)) static_for<g2_eh(M.v), g2_eh(M.v + 1)>([&](auto Z) {
            constexpr int z = Z.v;
            if constexpr (z < 24) { constexpr int st = z / 8, jj = (z >> 2) & 1, e = z & 3; acc[1][2 + jj][e] = i_add(acc[1][2 + jj][e], rr[2 * st + jj][e]); }
            else if constexpr (z < 28) { pr[z - 24] = pk_bf16(acc[1][2][z - 24], acc[1][3][z - 24]); PINV(pr[z - 24]); }
            else if constexpr (z < 36 && layer == 1 && (z & 1) == 0) OUT_ST((z - 28) >> 1);
          });
        }
        SB();
      });
      static_assert(g2_eh(224) >= 36, "the early half is complete");
      if constexpr (layer == 0) WAITV0();
      asm volatile("s_nop 7" ::: "memory");
      SB();
      *(LAS f32x4*)(pin1 + 2 * 1024) = acc[3][0];
      *(LAS f32x4*)(pin1 + 3 * 1024) = acc[3][1];
      LDS_BAR();
#pragma unroll
      for (int i = EH ? 1 : 0; i < 2; ++i) {
#pragma unroll
        for (int z = 0; z < 6; ++z) rr[z] = *(const LAS f32x4*)(pred + ((z >> 1) * 4 + 2 * i + (z & 1)) * 1024);
        SB();
        const f32x4 y0 = ((acc[2 * i + 1][2] + rr[0]) + rr[2]) + rr[4], y1 = ((acc[2 * i + 1][3] + rr[1]) + rr[3]) + rr[5];
#pragma unroll
        for (int k = 0; k < 4; ++k) pr[4 * i + k] = pk_bf16(y0[k], y1[k]);
      }
      if constexpr (layer == 1) {
#pragma unroll
        for (int k = EH ? 4 : 0; k < 8; ++k) OUT_ST(k);
      } else {
        const int cl = (vl >> 4) & 15;
        st32(san(__builtin_bit_cast(unsigned, stats16(pr, cl))), r0, OFF_PS2 + (g2 * 16 + 2 * db + dsel) * 256 + (vl >> 2));
        v4u o0, o1;
        tr8x2(pr, o0, o1);
        st128(o0, r0, LB_BYTES + LB_XIN + xro);
        st128(o1, r0, LB_BYTES + LB_XIN + xro + 1024);
        LDS_BAR();
        unsigned pq[16];
        auto pq_load = [&] {
#pragma unroll
          for (int i = 0; i < 16; ++i) pq[i] = ld32(r0, (vl >> 2) + i * 256, OFF_PS2 + g2 * 4096);
        };
        auto pq_late = [&] {
          unsigned mm = 0u;
#pragma unroll
          for (int i = 0; i < 8; ++i) mm = umax3(mm, pq[2 * i], pq[2 * i + 1]);
          return __any(mm == POISON);
        };
        expand_k(IC<0>{}, pq_load);
        SB();
#pragma unroll 1
        for (unsigned sp = 0; sp < SPIN_CAP && pq_late(); ++sp) {
          asm volatile("" ::: "memory");
          pq_load();
        }
        WAITV0();
        float tot[4];
#pragma unroll
        for (int i = 0; i < 4; ++i)
          tot[i] = (__builtin_bit_cast(float, pq[4 * i]) + __builtin_bit_cast(float, pq[4 * i + 1])) + (__builtin_bit_cast(float, pq[4 * i + 2]) + __builtin_bit_cast(float, pq[4 * i + 3]));
        float m, r;
        mean_rstd((tot[0] + tot[1]) + (tot[2] + tot[3]), cl, m, r);
        norm8(pr, m, r, o0, o1);
        st128(o0, r0, LB_BYTES + LB_UIN + xro);
        st128(o1, r0, LB_BYTES + LB_UIN + xro + 1024);
        if (U_ACK) WAITV0();
        if (U_DELAY) __builtin_amdgcn_s_sleep(U_DELAY);
      }
    }
  });
  asm volatile(".subsection 2\nk_code_end:\n\t.fill 128, 4, 0xBF800000\n\t.subsection 0");
}

extern "C" void kernel_launch(void* const* d_in, const int* in_sizes, int n_in, void* d_out, int out_size, void* d_ws, size_t ws_size,
                              hipStream_t stream) {
  Params p;
  p.x0 = (cus)d_in[0]; p.lar = (cus)d_in[1]; p.aim = (cus)d_in[2]; p.cr = (cus)d_in[3]; p.ci = (cus)d_in[4]; p.ldt = (cus)d_in[5];
  p.dsk = (cus)d_in[6]; p.w1 = (cus)d_in[8]; p.b1 = (cus)d_in[9]; p.w2 = (cus)d_in[10];
  p.ws = (u8*)d_ws; p.out = (unsigned*)d_out;
  (void)hipFuncSetAttribute((const void*)k_main, hipFuncAttributeMaxDynamicSharedMemorySize, LDS_TOTAL);
  k_main<<<256, 256, LDS_TOTAL, stream>>>(p);
}
```
